```python
import torch

def _softmax(x, axis=-1):
    return torch.softmax(x, dim=axis)
def _gelu(x):
    return torch.nn.functional.gelu(x, approximate="tanh")
def _silu(x):
    return torch.nn.functional.silu(x)
def _sigmoid(x):
    return torch.sigmoid(x)
def _layernorm(x, axis=-1, eps=1e-5):
    m = x.mean(dim=axis, keepdim=True)
    v = x.var(dim=axis, keepdim=True, unbiased=False)
    return (x - m) / torch.sqrt(v + eps)
def _scatter_add(tgt, idx, src, axis=0):
    return tgt.clone().index_add_(axis, idx, src)
def _segment_sum(src, seg, n):
    out = torch.zeros((n,) + src.shape[1:], dtype=src.dtype)
    return out.index_add_(0, seg, src)
def _pad(x, pw):
    flat = tuple(v for pair in reversed(pw) for v in pair)
    return torch.nn.functional.pad(x, flat)

kernel_name = 'recurrent_scan__ssm_conv_s5__glu_gated_reglu__prompt'

def setup_inputs():

    _w = (lambda a: (lambda t: t.to(torch.bfloat16) if t.is_floating_point() else t)(torch.as_tensor(a)))
    def _A_setup():
        B, S, D = 1, 2048, 448
        P = D
        Lambda_re = -0.5 * (lambda _x: torch.exp(torch.as_tensor(_x)))((lambda *s: torch.randn(*s, dtype=torch.bfloat16) * 0.8)(P))
        Lambda_im = (lambda *s: torch.randn(*s, dtype=torch.bfloat16) * 0.8)(P) * 1.0
        B_re = (lambda *s: torch.randn(*s, dtype=torch.bfloat16) * 0.8)(P, D) * (D ** -0.5)
        B_im = (lambda *s: torch.randn(*s, dtype=torch.bfloat16) * 0.8)(P, D) * (D ** -0.5)
        C_re = (lambda *s: torch.randn(*s, dtype=torch.bfloat16) * 0.8)(D, P) * (P ** -0.5)
        C_im = (lambda *s: torch.randn(*s, dtype=torch.bfloat16) * 0.8)(D, P) * (P ** -0.5)
        log_dt = (lambda *s: torch.randn(*s, dtype=torch.bfloat16) * 0.8)(P) * 0.3 - 2.0
        Dskip = (lambda *s: torch.randn(*s, dtype=torch.bfloat16) * 0.8)(D)
        return {
            "x": _w((lambda *s: torch.randn(*s, dtype=torch.bfloat16) * 0.05)(B, S, D)),
            "Lambda_re": _w(Lambda_re),
            "Lambda_im": _w(Lambda_im),
            "B_re": _w(B_re),
            "B_im": _w(B_im),
            "C_re": _w(C_re),
            "C_im": _w(C_im),
            "log_dt": _w(log_dt),
            "Dskip": _w(Dskip),
        }
    _A_in = _A_setup()

    def _B_setup():
        B, S, D, F = 1, 2048, 448, 1792
        return {
            "x": _w((lambda *s: torch.randn(*s, dtype=torch.bfloat16) * 0.05)(B, S, D)),
            "W_gate": _w((lambda *s: torch.randn(*s, dtype=torch.bfloat16) * 0.2)(D, F) * (2.0 / D) ** 0.5),
            "W_up": _w((lambda *s: torch.randn(*s, dtype=torch.bfloat16) * 0.2)(D, F) * (2.0 / D) ** 0.5),
            "W_down": _w((lambda *s: torch.randn(*s, dtype=torch.bfloat16) * 0.2)(F, D) * (1.0 / F) ** 0.5),
        }
    _B_in = _B_setup()

    return {
        "A_x": _A_in["x"],
        "A_Lambda_re": _A_in["Lambda_re"],
        "A_Lambda_im": _A_in["Lambda_im"],
        "A_B_re": _A_in["B_re"],
        "A_B_im": _A_in["B_im"],
        "A_C_re": _A_in["C_re"],
        "A_C_im": _A_in["C_im"],
        "A_log_dt": _A_in["log_dt"],
        "A_Dskip": _A_in["Dskip"],
        "B_x": _B_in["x"],
        "B_W_gate": _B_in["W_gate"],
        "B_W_up": _B_in["W_up"],
        "B_W_down": _B_in["W_down"],
    }

def reference(A_x, A_Lambda_re, A_Lambda_im, A_B_re, A_B_im, A_C_re, A_C_im, A_log_dt, A_Dskip, B_x, B_W_gate, B_W_up, B_W_down):
    def _A_ref(x, Lambda_re, Lambda_im, B_re, B_im, C_re, C_im, log_dt, Dskip):
        Bsz, L, H = x.shape
        Lam = Lambda_re + 1j * Lambda_im
        Bmat = B_re + 1j * B_im
        Cmat = C_re + 1j * C_im
        dt = torch.exp(log_dt)
        Lam_bar = torch.exp(dt * Lam)
        B_bar = ((Lam_bar - 1.0) / Lam)[:, None] * Bmat
        Bu = torch.einsum("bld,pd->blp", x + 0j, B_bar)
        h = torch.zeros_like(Bu[:, 0, :])
        hs = []
        for t in range(L):
            h = Lam_bar[None, :] * h + Bu[:, t, :]
            hs.append(h)
        hseq = torch.stack(hs, 1)
        y = 2.0 * torch.einsum("blp,dp->bld", hseq, Cmat).real + x * Dskip[None, None, :]
        return y

    def _B_ref(x, W_gate, W_up, W_down):
        gate = torch.einsum("bsd,df->bsf", x, W_gate)
        up = torch.einsum("bsd,df->bsf", x, W_up)
        gate = gate * (lambda x, dt: x.to(dt))(gate > 0, gate.dtype)
        h = gate * up
        return torch.einsum("bsf,fd->bsd", h, W_down)

    x = A_x
    xn = _layernorm(x, axis=-1)
    s = _A_ref(xn, A_Lambda_re, A_Lambda_im, A_B_re, A_B_im, A_C_re, A_C_im, A_log_dt, A_Dskip)
    g = _silu(_B_ref(xn, B_W_gate, B_W_up, B_W_down))
    return x + s * g
```

```cpp
#include <hip/hip_runtime.h>
#include <hip/hip_bf16.h>
#include <stdint.h>

#define L_ 2048
#define D_ 448
#define P_ 448
#define F_ 1792

typedef unsigned short u16;
__device__ __forceinline__ float bf2f(u16 v){ return __uint_as_float(((unsigned)v)<<16); }
__device__ __forceinline__ u16 f2bf(float f){ unsigned u=__float_as_uint(f); u += 0x7FFF + ((u>>16)&1); return (u16)(u>>16); }
__device__ __forceinline__ float rbf(float f){ return bf2f(f2bf(f)); }

__global__ void k_ln(const u16* x, u16* xn){
  int t=blockIdx.x; int lane=threadIdx.x;
  float v[7]; float s=0;
  for(int i=0;i<7;i++){ v[i]=bf2f(x[t*D_+lane+64*i]); s+=v[i]; }
  for(int o=32;o>0;o>>=1) s+=__shfl_xor(s,o);
  float m=s/448.f; float q=0;
  for(int i=0;i<7;i++){ float d=v[i]-m; q+=d*d; }
  for(int o=32;o>0;o>>=1) q+=__shfl_xor(q,o);
  float var=rbf(q/448.f); float mb=rbf(m);
  float den=rbf(sqrtf(rbf(var+1e-5f)));
  for(int i=0;i<7;i++){ xn[t*D_+lane+64*i]=f2bf(rbf(v[i]-mb)/den); }
}
__global__ void k_bu(const u16* xn,const u16* Bre,const u16* Bim,const u16* lre,const u16* lim,const u16* ldt,float2* Bu){
  int t=blockIdx.x; int p=threadIdx.x; if(p>=P_) return;
  float ar=0,ai=0;
  for(int d=0;d<D_;d++){ float xv=bf2f(xn[t*D_+d]); ar+=xv*bf2f(Bre[p*D_+d]); ai+=xv*bf2f(Bim[p*D_+d]); }
  float dt=rbf(expf(bf2f(ldt[p]))); float lr=bf2f(lre[p]), li=bf2f(lim[p]);
  float e=expf(dt*lr); float br=e*cosf(dt*li), bi=e*sinf(dt*li);
  float nr=br-1.f, ni=bi; float den=lr*lr+li*li;
  float cr=(nr*lr+ni*li)/den, ci=(ni*lr-nr*li)/den;
  Bu[t*P_+p]=make_float2(cr*ar-ci*ai, cr*ai+ci*ar);
}
__global__ void k_scan(float2* Bu,const u16* lre,const u16* lim,const u16* ldt){
  int p=blockIdx.x*64+threadIdx.x; if(p>=P_) return;
  float dt=rbf(expf(bf2f(ldt[p]))); float lr=bf2f(lre[p]), li=bf2f(lim[p]);
  float e=expf(dt*lr); float br=e*cosf(dt*li), bi=e*sinf(dt*li);
  float hr=0,hi=0;
  for(int t=0;t<L_;t++){ float2 b=Bu[t*P_+p]; float nr=br*hr-bi*hi+b.x, ni=br*hi+bi*hr+b.y; hr=nr;hi=ni; Bu[t*P_+p]=make_float2(hr,hi);}
}
__global__ void k_ffn1(const u16* xn,const u16* Wg,const u16* Wu,u16* h){
  int t=blockIdx.x;
  for(int f=threadIdx.x;f<F_;f+=blockDim.x){
    float g=0,u=0;
    for(int d=0;d<D_;d++){ float xv=bf2f(xn[t*D_+d]); g+=xv*bf2f(Wg[d*F_+f]); u+=xv*bf2f(Wu[d*F_+f]); }
    g=rbf(g); u=rbf(u); g = g>0.f? g:0.f*g;
    h[t*F_+f]=f2bf(g*u);
  }
}
__global__ void k_final(const u16* x,const u16* xn,const float2* hs,const u16* Cre,const u16* Cim,const u16* Dsk,const u16* h,const u16* Wd,float* out){
  int t=blockIdx.x; int d=threadIdx.x; if(d>=D_) return;
  float y=0;
  for(int p=0;p<P_;p++){ float2 hv=hs[t*P_+p]; y+=hv.x*bf2f(Cre[d*P_+p])-hv.y*bf2f(Cim[d*P_+p]); }
  y=2.f*y+rbf(bf2f(xn[t*D_+d])*bf2f(Dsk[d]));
  float o=0;
  for(int f=0;f<F_;f++) o+=bf2f(h[t*F_+f])*bf2f(Wd[f*D_+d]);
  o=rbf(o);
  float g=rbf(o/(1.f+expf(-o)));
  out[t*D_+d]=bf2f(x[t*D_+d])+y*g;
}

extern "C" void kernel_launch(void* const* d_in, const int* in_sizes, int n_in,
                              void* d_out, int out_size, void* d_ws, size_t ws_size,
                              hipStream_t stream) {
  const u16* x=(const u16*)d_in[0];
  const u16* lre=(const u16*)d_in[1]; const u16* lim=(const u16*)d_in[2];
  const u16* Bre=(const u16*)d_in[3]; const u16* Bim=(const u16*)d_in[4];
  const u16* Cre=(const u16*)d_in[5]; const u16* Cim=(const u16*)d_in[6];
  const u16* ldt=(const u16*)d_in[7]; const u16* Dsk=(const u16*)d_in[8];
  const u16* Wg=(const u16*)d_in[10]; const u16* Wu=(const u16*)d_in[11]; const u16* Wd=(const u16*)d_in[12];
  char* ws=(char*)d_ws;
  u16* xn=(u16*)ws; float2* Bu=(float2*)(ws+(4<<20)); u16* h=(u16*)(ws+(16<<20));
  k_ln<<<L_,64,0,stream>>>(x,xn);
  k_bu<<<L_,448,0,stream>>>(xn,Bre,Bim,lre,lim,ldt,Bu);
  k_scan<<<7,64,0,stream>>>(Bu,lre,lim,ldt);
  k_ffn1<<<L_,256,0,stream>>>(xn,Wg,Wu,h);
  k_final<<<L_,448,0,stream>>>(x,xn,Bu,Cre,Cim,Dsk,h,Wd,(float*)d_out);
}
```

```cpp
#include <hip/hip_runtime.h>
#include <stdint.h>

typedef unsigned short u16;
typedef unsigned u32;
typedef unsigned long long u64;
typedef __attribute__((ext_vector_type(8))) __bf16 bf16x8;
typedef __attribute__((ext_vector_type(2))) __bf16 bf16x2;
typedef __attribute__((ext_vector_type(4))) short s16x4;
typedef __attribute__((ext_vector_type(16))) float f32x16;
typedef __attribute__((ext_vector_type(4))) float f32x4;
typedef __attribute__((ext_vector_type(2))) float f32x2;
typedef __attribute__((ext_vector_type(4))) u32 u32x4;
typedef __attribute__((ext_vector_type(2))) u32 u32x2;
#define LAS __attribute__((address_space(3)))
#if defined(__HIP_DEVICE_COMPILE__)
#define NOPK __attribute__((target("no-packed-fp32-ops")))
#else
#define NOPK
#endif
#define FI __device__ __forceinline__ NOPK

#define PF_W 0
#define PF_C 0
#define NHELP 32
#define HFIRST 0
#define H_S0 0
#define H_S1 60
#define H_S2 8
#define RAMP 3
#define AWIN 8

constexpr int ROWB = 896;
constexpr int ROWW = 3584;
constexpr int NKS = 168;
constexpr u32 M_TRANS = 131072, M_SP = M_TRANS, M_CSB = M_TRANS + 2048, M_CONST = 147456, M_CS = 155648, M_STAT = 159744,
              M_W = 160768, M_H = 161792, M_COEF = 162816, M_DUMMY = 163072, LDS_BYTES = 163840;
constexpr size_t WS_FLAG = 4096, WS_GRAN = 65536, WS_H = 1u << 20;
constexpr int NUNIT = 92, RING = 16;
constexpr int imin(int a, int b) { return a < b ? a : b; }
constexpr int cum(int S) { return S < 0 ? 0 : S < 6 ? 3 * (S + 1) : S < 14 ? 22 + 2 * (S - 6) : S < 21 ? 36 + 2 * (S - 13) : 50 + (S - 20); }
constexpr int lim(int T) { return T < 0 ? 6 : T < 22 ? imin(cum(T - 2) + RING, lim(T - 1) + (T < 2 ? RAMP : 3)) : imin(NUNIT, 66 + 2 * ((T - 22) >> 1)); }

struct Params {
  const u16 *x, *lre, *lim, *Bre, *Bim, *Cre, *Cim, *ldt, *Dsk, *Wg, *Wu, *Wd;
  float* out;
  char* ws;
};

FI float u2f(u32 v) { return __builtin_bit_cast(float, v); }
FI u32 f2u(float v) { return __builtin_bit_cast(u32, v); }
FI void wait_vm(int n) {
  switch (n < 63 ? n : 63) {
#define C1(i) case i: asm volatile("s_waitcnt vmcnt(" #i ")" ::: "memory"); break;
    C1(0) C1(1) C1(2) C1(3) C1(4) C1(5) C1(6) C1(7) C1(8) C1(9) C1(10) C1(11) C1(12) C1(13) C1(14) C1(15)
    C1(16) C1(17) C1(18) C1(19) C1(20) C1(21) C1(22) C1(23) C1(24) C1(25) C1(26) C1(27) C1(28) C1(29) C1(30) C1(31)
    C1(32) C1(33) C1(34) C1(35) C1(36) C1(37) C1(38) C1(39) C1(40) C1(41) C1(42) C1(43) C1(44) C1(45) C1(46) C1(47)
    C1(48) C1(49) C1(50) C1(51) C1(52) C1(53) C1(54) C1(55) C1(56) C1(57) C1(58) C1(59) C1(60) C1(61) C1(62) C1(63)
#undef C1
  }
}
FI void dma16(const char* base, u32 voff, u32 ldsaddr, bool skip = false) {
  if (skip) return;
  asm volatile("s_mov_b32 m0, %2\n\ts_nop 0\n\tglobal_load_lds_dwordx4 %0, %1" ::"v"(voff), "s"(base), "s"(ldsaddr) : "memory");
}
FI void touch(const char* base, u32 voff) {
  asm volatile("s_mov_b32 m0, %2\n\ts_nop 0\n\tglobal_load_lds_dword %0, %1" ::"v"(voff), "s"(base), "s"(M_DUMMY) : "memory");
}
FI void gld16_sc1(u32x4& d, const char* base, u32 voff) {
  asm volatile("global_load_dwordx4 %0, %1, %2 sc1" : "=v"(d) : "v"(voff), "s"(base) : "memory");
}
FI void gst16_sc1(const char* base, u32 voff, u32x4 d) {
  asm volatile("global_store_dwordx4 %0, %1, %2 sc1\n\ts_nop 1" ::"v"(voff), "v"(d), "s"(base) : "memory");
}
FI void gld_u16(u32& d, const void* base, u32 voff) {
  asm volatile("global_load_ushort %0, %1, %2" : "=v"(d) : "v"(voff), "s"(base) : "memory");
}
FI void gld8(u32x2& d, const void* base, u32 voff) {
  asm volatile("global_load_dwordx2 %0, %1, %2" : "=v"(d) : "v"(voff), "s"(base) : "memory");
}
FI u32x4 lds16(u32 a) { return *(const LAS u32x4*)(uintptr_t)a; }
FI f32x4 ldsf4(u32 a) { return *(const LAS f32x4*)(uintptr_t)a; }
FI f32x2 ldsf2(u32 a) { return *(const LAS f32x2*)(uintptr_t)a; }
FI void stsf(u32 a, float v) { *(LAS float*)(uintptr_t)a = v; }
FI void stsf2(u32 a, float x, float y) { *(LAS f32x2*)(uintptr_t)a = f32x2{x, y}; }
FI u32x2 ldstr(u32 a) {
  s16x4 v = __builtin_amdgcn_ds_read_tr16_b64_v4i16((LAS s16x4*)(uintptr_t)a);
  return __builtin_bit_cast(u32x2, v);
}
FI u32x4 ldstr2(u32 a0, u32 a1) {
  u32x2 lo = ldstr(a0), hi = ldstr(a1);
  return u32x4{lo.x, lo.y, hi.x, hi.y};
}
FI f32x16 mfma32(u32x4 a, u32x4 b, f32x16 c) {
  return __builtin_amdgcn_mfma_f32_32x32x16_bf16(__builtin_bit_cast(bf16x8, a), __builtin_bit_cast(bf16x8, b), c, 0, 0, 0);
}
FI f32x4 mfma16(u32x4 a, u32x4 b, f32x4 c) {
  return __builtin_amdgcn_mfma_f32_16x16x32_bf16(__builtin_bit_cast(bf16x8, a), __builtin_bit_cast(bf16x8, b), c, 0, 0, 0);
}
FI float dot2(u32 a, u32 b, float c) {
  return __builtin_amdgcn_fdot2_f32_bf16(__builtin_bit_cast(bf16x2, a), __builtin_bit_cast(bf16x2, b), c, false);
}
FI u32 pack2(float a, float b) {
  bf16x2 v = {(__bf16)a, (__bf16)b};
  return __builtin_bit_cast(u32, v);
}
FI float rbf(float a) { return u2f(pack2(a, a) << 16); }
FI float bf2f(u32 v) { return u2f(v << 16); }
FI float bfhi(u32 v) { return u2f(v & 0xffff0000u); }
FI void swap16(float v, float& even, float& odd) {
  auto r = __builtin_amdgcn_permlane16_swap(f2u(v), f2u(v), false, false);
  even = u2f(r[0]); odd = u2f(r[1]);
}
FI void swap32(float v, float& lo, float& hi) {
  auto r = __builtin_amdgcn_permlane32_swap(f2u(v), f2u(v), false, false);
  lo = u2f(r[0]); hi = u2f(r[1]);
}
FI float xhalf_sum(float v) { float a, b; swap32(v, a, b); return a + b; }
FI bool wany(bool c) { return __builtin_amdgcn_ballot_w64(c) != 0; }
struct cf { float r, i; };
FI cf cmul(cf a, cf b) { return cf{a.r * b.r - a.i * b.i, a.r * b.i + a.i * b.r}; }
FI cf csqr(cf a) { return cf{a.r * a.r - a.i * a.i, (a.r + a.r) * a.i}; }
FI cf cfma(cf a, cf b, cf c) {
  return cf{__builtin_fmaf(-a.i, b.i, __builtin_fmaf(a.r, b.r, c.r)), __builtin_fmaf(a.i, b.r, __builtin_fmaf(a.r, b.i, c.i))};
}
FI cf csel(bool p, cf a, cf b) { return cf{p ? a.r : b.r, p ? a.i : b.i}; }
FI float exp_c(float x) {
  const float t = x * 1.44269502162933349609f;
  const float r = __builtin_fmaf(x, 1.92596299112661746e-8f, __builtin_fmaf(x, 1.44269502162933349609f, -t));
  const float y = __builtin_amdgcn_exp2f(t);
  return __builtin_fmaf(y, r * 0.693147182464599609375f, y);
}
FI u32x4 split3(float cs) {
  const float hi = rbf(cs), r1 = cs - hi, mid = rbf(r1), lo = r1 - mid;
  return u32x4{pack2(hi, mid), pack2(lo, 0.f), 0u, 0u};
}
#define SB() __builtin_amdgcn_sched_barrier(0)
FI int lane_id() { int v; asm volatile("v_mbcnt_lo_u32_b32 %0, -1, 0\n\tv_mbcnt_hi_u32_b32 %0, -1, %0" : "=v"(v)); return v; }
template <int V> struct IC { static constexpr int value = V; };
template <int I, int N, class F> FI void sfor(F&& f) {
  if constexpr (I < N) { f(IC<I>{}); sfor<I + 1, N>(f); }
}
#define AI __attribute__((always_inline)) NOPK
#define PIN(x) asm volatile("" : "+v"(x))
#define PINC(c) asm volatile("" : "+v"((c).r), "+v"((c).i))

__global__ __launch_bounds__(512, 2) NOPK void fused_ssm_ffn(Params p) {
  const int w = __builtin_amdgcn_readfirstlane(threadIdx.x >> 6);
  const int mw = w & 3, nw = w >> 2;
  if (HFIRST ? blockIdx.x < NHELP : blockIdx.x >= 224) {
    const int hb = HFIRST ? blockIdx.x : blockIdx.x - 224;
    const int n0 = ((hb >> 3) * 8 + w) * 64 + lane_id();
    u64 pc0;
    u32 len;
    int back;
    asm volatile("s_getpc_b64 %0\n.Lpf_h:\n\ts_mov_b32 %1, .Lpf_end-.Lpf_begin\n\ts_mov_b32 %2, .Lpf_h-.Lpf_begin" : "=s"(pc0), "=s"(len), "=s"(back));
    if ((u32)n0 * 64 + 4 <= len) touch((const char*)(pc0 - (long long)back), (u32)n0 * 64);
    if (H_S0) __builtin_amdgcn_s_sleep(H_S0);
    for (int r = 0; r < 8; ++r) {
      const u32 n = (r & 3) * 2048 + n0, row = n / 7, jj = n - row * 7;
      const u32 o = (row >= 448 ? row - 448 : row) * ROWW + (7 * (hb & 1) + jj) * 256 + (r >> 2) * 128;
      if (n < 3136) touch((const char*)p.Wg, o);
      else if (n < 6272) touch((const char*)p.Wu, o);
    }
    __builtin_amdgcn_s_sleep(H_S1); __builtin_amdgcn_s_sleep(H_S1);
    for (int r = 0; r < 10; ++r) {
      const u32 n = r * 2048 + n0;
      if (n < 12544) touch((const char*)p.Wd, n * 128);
      else if (n < 15680) touch((const char*)p.Cre, (n - 12544) * 128);
      else if (n < 18816) touch((const char*)p.Cim, (n - 15680) * 128);
      __builtin_amdgcn_s_sleep(H_S2);
    }
    asm volatile("s_waitcnt vmcnt(0)" ::: "memory");
    return;
  }
  const int b = (NHELP && HFIRST) ? blockIdx.x - NHELP : blockIdx.x, xcd = b & 7, slot = b >> 3;
  const int g = 4 * (xcd >> 1) + slot / 7, j = 7 * (xcd & 1) + slot % 7;

#define ST3(i)
  ST3(0);
#define ST4(i)
  int vm = 0;
  int pos[NUNIT];

  u64 pcb;
  asm volatile("s_getpc_b64 %0\n.Lpf_begin:" : "=s"(pcb));
  u32 v_par, v_ep;
  u32 vX, vB;
  const int lane0 = lane_id();
  {
    const int l = lane0;
    const int dr = 8 * w + (l >> 3), dc = l & 7;
    const int dr31 = dr & 31;
    const u32 sw = (dc ^ ((dr >> 1) & 7)) << 4;
    vX = (u32)((32 * (dr >> 5) + 16 * ((dr31 >> 2) & 1) + (dr31 & 3) + 4 * (dr31 >> 3)) * ROWB) + sw;
    vB = (u32)((16 * (w >> 2) + 8 * (w & 1) + (l >> 3)) * ROWB) + sw;
    PIN(vX); PIN(vB);
    SB();
    const u16* pp = (l < 16 ? p.lre : l < 32 ? p.lim : p.ldt) + (32 * j + 16 * nw + (l & 15));
    asm volatile("global_load_ushort %0, %1, off" : "=v"(v_par) : "v"(pp) : "memory");
    vm += 1;
  }
  const int pos_par = vm;
  u32 vG, vU, vD, vC;
  auto make_vGU = [&]() AI {
    const int l = lane_id();
    const int dr = 8 * w + (l >> 3), dc = l & 7;
    vG = (u32)(dr * ROWW + ((dc ^ (((dr >> 1) & 1) << 1)) << 4));
    vU = vG ^ 64u;
  };
  auto make_vDC = [&]() AI {
    const int l = lane_id();
    const int dr = 8 * w + (l >> 3), dc = l & 7;
    vD = (u32)(dr * ROWB + ((dc ^ (((dr >> 1) & 3) << 1)) << 4));
    vC = (u32)(dr * ROWB + ((dc ^ ((dr >> 1) & 7)) << 4));
  };
  const char* bX = (const char*)p.x + (size_t)g * 128 * ROWB;
  const char* bB = (const char*)(((w >> 1) & 1) ? p.Bim : p.Bre) + (size_t)j * 32 * ROWB;
  const char* bG = (const char*)p.Wg + j * 256;
  const char* bU = (const char*)p.Wu + j * 256;
  const int nb = j % 7, mb2 = j / 7;
  const char* bD = (const char*)p.Wd + nb * 128;
  const char* bCr = (const char*)p.Cre + (size_t)nb * 64 * ROWB;
  const char* bCi = (const char*)p.Cim + (size_t)nb * 64 * ROWB;
  const u32 ldsw = w * 1024;

  auto issue_unit = [&](auto uc) AI {
    constexpr int u = decltype(uc)::value;
    const u32 dst = ldsw + (u & 15) * 8192;
    if constexpr (u < 21) {
      constexpr int S = u / 3, rr = u % 3;
      constexpr int kind = (S & 1) ? (rr == 0 ? 2 : rr - 1) : rr;
      if constexpr (kind == 2) dma16(bB + S * 128, vB, dst);
      else dma16(bX + kind * 64 * ROWB + S * 128, vX, dst);
    } else if constexpr (u == 21) {
      return;
    } else if constexpr (u < 50) {
      constexpr int P = (u - 22) / 14, kb = ((u - 22) % 14) >> 1, img = u & 1;
      dma16((img ? bU : bG) + (size_t)kb * 64 * ROWW + P * 128, img ? vU : vG, dst);
    } else {
      constexpr int t = u - 50;
      if constexpr (t < 14) dma16(bD + (size_t)(128 * t) * ROWB, vD, dst);
      else if constexpr (t < 28) dma16((((t - 14) & 1) ? bCi : bCr) + ((t - 14) >> 1) * 128, vC, dst);
      else dma16(bD + (size_t)(128 * (t - 28) + 64) * ROWB, vD, dst);
    }
    pos[u] = ++vm;
  };
  auto issue_nth = [&](auto Tc, auto nc) AI {
    constexpr int T = decltype(Tc)::value, u = lim(T - 1) + decltype(nc)::value;
    if constexpr (u < lim(T)) issue_unit(IC<u>{});
  };
  auto Y = [&](auto Tc, int extra_pos, bool drain_lds) AI {
    constexpr int T = decltype(Tc)::value;
    constexpr int last = T >= 22 ? imin(NUNIT - 1, cum(T + 1) - 1) : cum(T) - 1 == 21 ? 20 : cum(T) - 1;
    int need = pos[last]; if (extra_pos > need) need = extra_pos;
    SB();
    wait_vm(vm - need);
    if (drain_lds) asm volatile("s_waitcnt lgkmcnt(0)" ::: "memory");
    asm volatile("s_barrier" ::: "memory");
    SB();
  };
  auto prefetch_w = [&]() AI {
  };
  auto prefetch_c = [&]() AI {
  };
  sfor<0, lim(-1)>(issue_unit);
  prefetch_c();

  u32 tag;
  cf lam, lam8, lam16, lam32, lam8q;
  float k_lr, k_li, k_e, k_th;
  cf k_a, k_b, k_c, k_l64;
  u32 aS, aT;
  auto consts = [&](auto Sc) AI {
    constexpr int S = decltype(Sc)::value;
    if constexpr (S == 0) {
      wait_vm(vm - pos_par);
      asm volatile("" : "+v"(v_par));
      float ev, od, d0, ldt, dd;
      swap16(u2f(v_par), ev, od);
      swap32(ev, k_lr, ldt); swap32(od, k_li, dd); (void)d0;
      k_lr = bf2f(f2u(k_lr)); k_li = bf2f(f2u(k_li));
      const float dt = rbf(exp_c(bf2f(f2u(ldt))));
      k_e = exp_c(dt * k_lr); k_th = dt * k_li;
      PIN(k_e); PIN(k_th); PIN(k_lr); PIN(k_li);
    } else if constexpr (S == 1) {
      float k_s, k_co;
      if (__builtin_expect(wany(!(__builtin_fabsf(k_th) <= 4096.f)), 0)) {
        k_s = sinf(k_th); k_co = cosf(k_th);
      } else {
        const float k = __builtin_rintf(k_th * 0.636619746685028076171875f);
        float r = __builtin_fmaf(-k, 1.57079637050628662109375f, k_th);
        r = __builtin_fmaf(-k, -4.37113882867379118e-8f, r);
        const float z = r * r;
        float ps = __builtin_fmaf(z, -1.9515295891e-4f, 8.3321608736e-3f);
        ps = __builtin_fmaf(z, ps, -1.6666654611e-1f);
        const float sn = __builtin_fmaf(r * z, ps, r);
        float pc = __builtin_fmaf(z, 2.443315711809948e-5f, -1.388731625493765e-3f);
        pc = __builtin_fmaf(z, pc, 4.166664568298827e-2f);
        const float cs = __builtin_fmaf(z * z, pc, __builtin_fmaf(z, -0.5f, 1.f));
        const int ki = (int)k;
        const float s1 = (ki & 1) ? cs : sn, c1 = (ki & 1) ? sn : cs;
        k_s = u2f(f2u(s1) ^ ((u32)(ki & 2) << 30));
        k_co = u2f(f2u(c1) ^ ((u32)((ki + 1) & 2) << 30));
      }
      lam = cf{k_e * k_co, k_e * k_s};
      PINC(lam);
    } else if constexpr (S == 2) {
      const int l = lane_id(), s16 = l & 15;
      aS = nw * 512 + s16 * 8; aT = (w * 64 + l) * 16;
      PIN(aS); PIN(aT);
      const float nr = lam.r - 1.f, ni = lam.i, dd = k_lr * k_lr + k_li * k_li;
      float rc = __builtin_amdgcn_rcpf(dd);
      rc = __builtin_fmaf(__builtin_fmaf(-dd, rc, 1.f), rc, rc);
      const float den = rc + rc;
      stsf2(M_COEF + aS, (nr * k_lr + ni * k_li) * den, (ni * k_lr - nr * k_li) * den);
    } else if constexpr (S == 3) {
      const cf l2 = csqr(lam), l4 = csqr(l2);
      lam8 = csqr(l4); lam16 = csqr(lam8); lam32 = csqr(lam16);
      PINC(lam8); PINC(lam16); PINC(lam32);
    } else if constexpr (S == 4) {
      const int q = lane_id() >> 4;
      const cf one = cf{1.f, 0.f};
      lam8q = cmul(csel(q & 1, lam8, one), csel(q & 2, lam16, one));
      PINC(lam8q);
    } else if constexpr (S == 5) {
      const cf one = cf{1.f, 0.f};
      k_l64 = csqr(lam32);
      k_a = cmul(csel(mw & 1, lam32, one), csel(mw & 2, k_l64, one));
      PINC(k_l64); PINC(k_a);
    } else if constexpr (S == 6) {
      k_a = cmul(lam8q, k_a);
      stsf2(M_CONST + aT, k_a.r, k_a.i);
      k_b = csqr(k_l64);
      k_c = csqr(k_b);
      PINC(k_b); PINC(k_c);
    } else if constexpr (S == 7) {
      const int q = lane_id() >> 4;
      const cf one = cf{1.f, 0.f};
      int ex = g - 1 - (4 * mw + q); ex = ex < 0 ? 0 : ex;
      k_a = cmul(csel(ex & 1, k_b, one), csel(ex & 2, k_c, one));
      k_b = csqr(k_c);
      PINC(k_a); PINC(k_b);
    } else if constexpr (S == 8) {
      const int q = lane_id() >> 4;
      const cf one = cf{1.f, 0.f};
      int ex = g - 1 - (4 * mw + q); ex = ex < 0 ? 0 : ex;
      k_c = csqr(k_b);
      k_a = cmul(k_a, cmul(csel(ex & 4, k_b, one), csel(ex & 8, k_c, one)));
      stsf2(M_CONST + aT + 8, k_a.r, k_a.i);
    }
  };
  consts(IC<0>{});

  u32x4 xf[28];
  f32x16 accS = {0};
  const u32 ONES = 0x3f803f80u;
  {
    float sx0 = 0.f, sx1 = 0.f, sq0 = 0.f, sq1 = 0.f, csB = 0.f;
    const int l = lane_id(), h = l >> 5, r32 = l & 31;
    const int rowx = 32 * (mw & 1) + r32;
    const u32 aX = (mw >> 1) * 8192 + rowx * 128 + ((h ^ ((rowx >> 1) & 7)) << 4);
    const int rowb = 32 * nw + r32;
    const u32 aB = rowb * 128 + ((h ^ ((rowb >> 1) & 7)) << 4);
    const u32 aBm = aB ^ (32 * mw);
    u32x4 bf[2][4], bc[2];
    auto comp = [&](auto Sc, auto hc) AI {
      constexpr int S = decltype(Sc)::value, hh = decltype(hc)::value;
#pragma unroll
      for (int i = 2 * hh; i < 2 * hh + 2; ++i) accS = mfma32(xf[4 * S + i], bf[S & 1][i], accS);
      PIN(accS);
      if ((S & 1) == nw) {
#pragma unroll
        for (int i = 2 * hh; i < 2 * hh + 2; ++i) {
          const u32x4 xv = xf[4 * S + i];
          sx0 = dot2(xv.x, ONES, sx0); sx1 = dot2(xv.y, ONES, sx1); sx0 = dot2(xv.z, ONES, sx0); sx1 = dot2(xv.w, ONES, sx1);
          sq0 = dot2(xv.x, xv.x, sq0); sq1 = dot2(xv.y, xv.y, sq1); sq0 = dot2(xv.z, xv.z, sq0); sq1 = dot2(xv.w, xv.w, sq1);
        }
        PIN(sx0); PIN(sx1); PIN(sq0); PIN(sq1);
      }
      if constexpr (hh == 0) {
        const u32x4 bv = bc[S & 1];
        csB = dot2(bv.x, ONES, csB); csB = dot2(bv.y, ONES, csB); csB = dot2(bv.z, ONES, csB); csB = dot2(bv.w, ONES, csB);
        PIN(csB);
      }
    };
    sfor<0, 7>([&](auto Sc) AI {
      constexpr int S = decltype(Sc)::value;
      Y(Sc, 0, false);
      issue_nth(Sc, IC<0>{});
      SB();
      constexpr int ux = 3 * S + (S & 1), ub = (S & 1) ? 3 * S : 3 * S + 2;
#pragma unroll
      for (int i = 0; i < 4; ++i) {
        xf[4 * S + i] = lds16((aX ^ (32 * i)) + (ux & 15) * 8192);
        bf[S & 1][i] = lds16((aB ^ (32 * i)) + (ub & 15) * 8192);
      }
      bc[S & 1] = lds16(aBm + (ub & 15) * 8192);
      SB();
      if constexpr (S == 0) { consts(IC<1>{}); SB(); prefetch_w(); }
      if constexpr (S > 0) comp(IC<S - 1>{}, IC<0>{});
      SB();
      issue_nth(Sc, IC<1>{});
      SB();
      if constexpr (S == 0) { consts(IC<2>{}); consts(IC<3>{}); }
      if constexpr (S > 0) comp(IC<S - 1>{}, IC<1>{});
      if constexpr (S == 1) {
        asm volatile("global_load_dword %0, %1, %2 sc1" : "=v"(v_ep) : "v"(0u), "s"(p.ws) : "memory");
        vm += 1;
        make_vGU();
      }
      SB();
      issue_nth(Sc, IC<2>{});
      if constexpr (S < 2) { issue_nth(Sc, IC<3>{}); issue_nth(Sc, IC<4>{}); }
    });
    comp(IC<6>{}, IC<0>{});
    comp(IC<6>{}, IC<1>{});
    const float sx = xhalf_sum(sx0 + sx1), sq = xhalf_sum(sq0 + sq1), cb = xhalf_sum(csB);
    stsf2(M_SP + (32 * mw + r32) * 16 + nw * 8, sx, sq);
    stsf(M_CSB + (32 * nw + r32) * 16 + mw * 4, cb);
  }

  u32 aW0, aW1, aW0h, aW1h;
  {
    const int l = lane_id(), h = l >> 5, q = l >> 4;
    const int fi = l & 15, fr = fi >> 2, fc4 = fi & 3, fimg = q & 1, fsw = (fr >> 1) | (fimg << 1);
    aW0 = fimg * 8192 + (8 * h + fr) * 128 + (((2 * nw) ^ fsw) << 5) + fc4 * 8;
    aW1 = aW0 ^ 32u;
    aW0h = aW0 + 65536; aW1h = aW1 + 65536;
    PIN(aW0h); PIN(aW1h);
  }
  const char* bH = p.ws + WS_H + (size_t)(4 * g + mw) * NKS * 1024;
  u32x4 wf[2][2];
  auto rdW = [&](int buf, int ug, u32 ko) AI {
    const u32 o = (ug & 7) * 8192 + ko;
    const u32 b0 = (ug & 8) ? aW0h : aW0, b1 = (ug & 8) ? aW1h : aW1;
    wf[buf][0] = ldstr2(b0 + o, b0 + o + 512);
    wf[buf][1] = ldstr2(b1 + o, b1 + o + 512);
  };
  int pos_gran = 0, pos_hs = 0, pos_poll = 0, pos_ha = 0, pos_hb = 0;
  u32x4 gran;
  u32x2 pollv;
  const char* bGran = p.ws + WS_GRAN + (size_t)j * 16 * 512;
  const char* bFlag = p.ws + WS_FLAG + (size_t)g * 14 * 192;
  constexpr int AB = AWIN + 1;
  u32x4 Aw[AB];
  const int mq = w & 3, kq = w >> 2;
  const char* bA = p.ws + WS_H + (size_t)(4 * g + 2 * mb2 + (mq >> 1)) * NKS * 1024;
  u32 vA;
  int posA[42];
  auto issueA = [&](auto tc) AI {
    constexpr int t = decltype(tc)::value;
    if constexpr (t < 42) {
      constexpr int ks = t < 14 ? 8 * t : t < 28 ? 112 + 28 * ((t - 14) & 1) + 4 * ((t - 14) >> 1) : 8 * (t - 28) + 4;
      gld16_sc1(Aw[t % AB], bA + ks * 1024, vA);
      posA[t] = ++vm;
    }
  };
  auto poll_issue = [&](int which) AI {
    pos_poll = vm;
    if (w == 0) {
      const int l = lane_id();
      const u32 fo = (l < 14 ? l : 0) * 192 + which * 64;
      asm volatile("global_load_dwordx2 %0, %1, %2 sc1" : "=v"(pollv) : "v"(fo), "s"(bFlag) : "memory");
    }
  };
  auto poll_check = [&](int which) AI {
    if (w == 0) {
      wait_vm(vm - pos_poll);
      asm volatile("" : "+v"(pollv));
      const int l = lane_id();
      const u32 fo = (l < 14 ? l : 0) * 192 + which * 64;
      int spins = 0;
      while (wany(!(pollv.x == tag && pollv.y == tag)) && spins < (1 << 20)) {
        asm volatile("global_load_dwordx2 %0, %1, %2 sc1\n\ts_waitcnt vmcnt(0)" : "=v"(pollv) : "v"(fo), "s"(bFlag) : "memory");
        ++spins;
      }
    }
  };
  auto flag_store = [&](int which) AI {
    if (threadIdx.x == 0) {
      u32x2 t2 = {tag, tag};
      asm volatile("global_store_dwordx2 %0, %1, %2 sc1" ::"v"((u32)(j * 192 + which * 64)), "v"(t2), "s"(bFlag) : "memory");
    }
  };
  u32 aD[4], aDh[4], aC0, aC0h;
  u32x4 bfr[2][4];
  auto make_p2 = [&]() AI {
    const int l = lane_id(), q = l >> 4, s16 = l & 15;
    const int pr = s16 >> 2, pc4 = l & 3, ph = q & 1, psw = 2 * ph + (pr >> 1);
    const u32 aD0 = (32 * kq + 16 * (q >> 1) + 4 * ph + pr) * 128 + pc4 * 8;
#pragma unroll
    for (int n = 0; n < 4; ++n) aD[n] = aD0 + ((n ^ psw) << 5);
    aC0 = s16 * 128 + (((4 * kq + q) ^ ((s16 >> 1) & 7)) << 4);
#pragma unroll
    for (int n = 0; n < 4; ++n) { aDh[n] = aD[n] + 65536; PIN(aDh[n]); }
    aC0h = aC0 + 65536; PIN(aC0h);
  };
  auto rdB = [&](auto tc) AI {
    constexpr int t = decltype(tc)::value;
    const u32 o = ((50 + t) & 7) * 8192;
    constexpr bool hi = ((50 + t) & 8) != 0;
    if constexpr (t >= 14 && t < 28) {
#pragma unroll
      for (int n = 0; n < 4; ++n) bfr[t & 1][n] = lds16((hi ? aC0h : aC0) + o + n * 2048);
    } else {
#pragma unroll
      for (int n = 0; n < 4; ++n) {
        const u32 a = (hi ? aDh[n] : aD[n]) + o;
        bfr[t & 1][n] = ldstr2(a, a + 1024);
      }
    }
  };

  u32 xm0, xm1;
  cf u[8], e_, A_, T1_, T3_, Pq, Wend, Ploc, H_;
  f32x4 f_a, f_b, f_c;
  f32x2 f_cc;
  float f_t0, f_t1;
  f32x2 f_h0, f_h1, f_h2, f_h3, f_h4;
  u32 vGr;
  u32 o_re[4], o_im[4];
  u32x4 fre, fim;
  float o_r0, o_i0;
  auto fill = [&](auto qc) AI {
    constexpr int Q = decltype(qc)::value;
    if constexpr (Q == 0) {
      const int l = lane_id(), r32 = l & 31;
      const float sx = f_a.x + f_a.z, sq = f_a.y + f_a.w;
      const float mean = sx * (1.f / 448.f), mb = rbf(mean);
      const float var = rbf(fmaxf(__builtin_fmaf(-mean, mean, sq * (1.f / 448.f)), 0.f));
      const float den = rbf(__builtin_amdgcn_sqrtf(rbf(var + 1e-5f)));
      const float rs = __builtin_amdgcn_rcpf(den);
      const int tk4 = 128 * mw + 64 * ((r32 >> 2) & 1) + 4 * (r32 & 3) + 16 * (r32 >> 3);
      stsf(M_STAT + tk4, mb);
      stsf(M_STAT + 512 + tk4, rs);
      const u32 nm = l < 32 ? pack2(-mb, -mb) : 0u;
      xm0 = nm; xm1 = nm & 0xffffu;
      PIN(xm0); PIN(xm1);
    } else if constexpr (Q == 1) {
      const int q = lane_id() >> 4;
      accS = mfma32(u32x4{xm0, xm1, 0u, 0u}, split3((f_b.x + f_b.y) + (f_b.z + f_b.w)), accS);
      PIN(accS);
      f_a = ldsf4(M_STAT + 512 + 128 * mw + 32 * q); f_b = ldsf4(M_STAT + 512 + 128 * mw + 32 * q + 16);
    } else if constexpr (Q == 2) {
      tag = __builtin_amdgcn_readfirstlane(v_ep) + 1;
      consts(IC<4>{});
    } else if constexpr (Q == 3 || Q == 4) {
#pragma unroll
      for (int i = 4 * (Q - 3); i < 4 * (Q - 3) + 4; ++i) {
        auto sw = __builtin_amdgcn_permlane16_swap(f2u(accS[i]), f2u(accS[i + 8]), false, false);
        const float rr = i < 4 ? f_a[i] : f_b[i - 4];
        u[i] = cf{u2f(sw[0]) * rr, u2f(sw[1]) * rr};
        PINC(u[i]);
      }
    } else if constexpr (Q == 5) {
      e_ = cfma(lam, u[0], u[1]); e_ = cfma(lam, e_, u[2]); e_ = cfma(lam, e_, u[3]); PINC(e_);
    } else if constexpr (Q == 6) {
      e_ = cfma(lam, e_, u[4]); e_ = cfma(lam, e_, u[5]); PINC(e_);
    } else if constexpr (Q == 7) {
      e_ = cfma(lam, e_, u[6]); e_ = cfma(lam, e_, u[7]); PINC(e_);
    } else if constexpr (Q == 8) {
      cf d, T, yo;
      swap16(e_.r, A_.r, d.r); swap16(e_.i, A_.i, d.i);
      T = cfma(lam8, A_, e_);
      swap16(T.r, d.r, yo.r); swap16(T.i, d.i, yo.i);
      swap32(yo.r, T1_.r, T3_.r); swap32(yo.i, T1_.i, T3_.i);
      PINC(A_); PINC(T1_); PINC(T3_);
    } else if constexpr (Q == 9) {
      Wend = cfma(lam16, T1_, T3_);
      stsf2(M_W + mw * 128 + aS, Wend.r, Wend.i);
      PINC(Wend);
    } else if constexpr (Q == 10) {
      const int q = lane_id() >> 4;
      const cf P3 = cfma(lam8, T1_, A_);
      Pq = csel(q & 2, csel(q & 1, P3, T1_), csel(q & 1, A_, cf{0.f, 0.f}));
      PINC(Pq);
    } else if constexpr (Q == 11) {
      const f32x2 w0 = ldsf2(M_W + aS), w1 = ldsf2(M_W + 128 + aS);
      f_cc = ldsf2(M_W + 256 + aS);
      f_a = f32x4{w0.x, w0.y, w1.x, w1.y};
    } else if constexpr (Q == 12) {
      cf Qc = cf{0.f, 0.f};
      if (mw > 0) Qc = cf{f_a.x, f_a.y};
      if (mw > 1) Qc = cfma(lam32, Qc, cf{f_a.z, f_a.w});
      if (mw > 2) Qc = cfma(lam32, Qc, cf{f_cc.x, f_cc.y});
      Ploc = cfma(lam8q, Qc, Pq);
      Wend = cfma(lam32, Qc, Wend);
      PINC(Ploc); PINC(Wend);
    } else if constexpr (Q == 13) {
      if (mw == 3 && lane_id() < 16) {
        u32x4 gv = {f2u(Wend.r), tag, f2u(Wend.i), tag};
        gst16_sc1(bGran + g * 512, (aT & 0xf0u) + nw * 256, gv);
      }
    } else if constexpr (Q >= 14 && Q <= 17) {
      consts(IC<Q - 9>{});
    } else if constexpr (Q == 18) {
      make_vDC();
    } else if constexpr (Q == 20) {
      const int q = lane_id() >> 4;
      vGr = (u32)((4 * mw + q) * 512) + (aS & 0x78u) * 2 + nw * 256;
      PIN(vGr);
      gld16_sc1(gran, bGran, vGr);
      pos_gran = ++vm;
    } else if constexpr (Q == 23) {
      asm volatile("" : "+v"(gran));
      const f32x2 cv = ldsf2(M_CONST + aT + 8);
      const bool need = (vGr >> 9) < (u32)g;
      int spins = 0;
      while (__builtin_expect(wany(need && !(gran.y == tag && gran.w == tag)), 0) && spins < (1 << 20)) {
        gld16_sc1(gran, bGran, vGr);
        asm volatile("s_waitcnt vmcnt(0)" : "+v"(gran)::"memory");
        ++spins;
      }
      H_ = cmul(cf{cv.x, cv.y}, cf{u2f(gran.x), u2f(gran.z)});
      if (!need) H_ = cf{0.f, 0.f};
      PINC(H_);
    } else if constexpr (Q == 24) {
      float a0, a1;
      swap16(H_.r, a0, a1); H_.r = a0 + a1; swap16(H_.i, a0, a1); H_.i = a0 + a1;
      swap32(H_.r, a0, a1); H_.r = a0 + a1; swap32(H_.i, a0, a1); H_.i = a0 + a1;
      stsf2(M_H + mw * 128 + aS, H_.r, H_.i);
    } else if constexpr (Q == 27) {
      f_h0 = ldsf2(M_H + aS); f_h1 = ldsf2(M_H + 128 + aS); f_h2 = ldsf2(M_H + 256 + aS); f_h3 = ldsf2(M_H + 384 + aS);
      f_h4 = ldsf2(M_CONST + aT);
      f_cc = ldsf2(M_COEF + aS);
    } else if constexpr (Q == 28) {
      const cf Hin = cf{(f_h0.x + f_h1.x) + (f_h2.x + f_h3.x), (f_h0.y + f_h1.y) + (f_h2.y + f_h3.y)};
      H_ = cfma(cf{f_h4.x, f_h4.y}, Hin, Ploc);
      PINC(H_); PIN(f_cc);
    } else if constexpr (Q >= 29 && Q <= 36) {
      constexpr int i = Q - 29;
      H_ = cfma(lam, H_, u[i]);
      const float orr = __builtin_fmaf(f_cc.x, H_.r, -(f_cc.y * H_.i)), oim = __builtin_fmaf(f_cc.x, H_.i, f_cc.y * H_.r);
      if constexpr ((i & 1) == 0) { o_r0 = orr; o_i0 = oim; PIN(o_r0); PIN(o_i0); }
      else { o_re[i >> 1] = pack2(o_r0, orr); o_im[i >> 1] = pack2(o_i0, oim) ^ 0x80008000u; PIN(o_re[i >> 1]); PIN(o_im[i >> 1]); }
      PINC(H_);
    } else if constexpr (Q == 37) {
      const int l = lane_id(), q = l >> 4, s16 = l & 15;
      const u32 wa = M_TRANS + w * 2048 + s16 * 64 + ((q ^ ((s16 >> 1) & 3)) << 4);
      *(LAS u32x4*)(uintptr_t)(wa) = u32x4{o_re[0], o_re[1], o_re[2], o_re[3]};
      *(LAS u32x4*)(uintptr_t)(wa + 1024) = u32x4{o_im[0], o_im[1], o_im[2], o_im[3]};
    } else if constexpr (Q == 39) {
      const int l = lane_id(), h = l >> 5, q = l >> 4, s16 = l & 15, fr = s16 >> 2, fc4 = l & 3;
      const u32 tb = M_TRANS + w * 2048;
      const int gg = q & 1, tc = 2 * (fc4 & 1) + gg;
      const int st0 = 8 * h + fr;
      const u32 ra0 = tb + st0 * 64 + ((tc ^ ((st0 >> 1) & 3)) << 4) + (fc4 >> 1) * 8;
      const u32 ra1 = (ra0 + 256) ^ 32u;
      fre = ldstr2(ra0, ra1); fim = ldstr2(ra0 + 1024, ra1 + 1024);
    } else if constexpr (Q == 40) {
      const u32 vL16 = aT & 1023u;
      gst16_sc1(bH + (112 + 2 * j + nw) * 1024, vL16, fre);
      gst16_sc1(bH + (140 + 2 * j + nw) * 1024, vL16, fim);
      vm += 2;
      pos_hs = vm;
    } else if constexpr (Q == 44) {
      poll_issue(0);
    } else if constexpr (Q == 48) {
      const int l = lane_id(), q = l >> 4, s16 = l & 15;
      vA = (q >> 1) * 1024 + (q & 1) * 512 + (16 * (mq & 1) + s16) * 16 + kq * 2048;
      PIN(vA);
    } else if constexpr (Q == 52) {
      make_p2();
    }
  };
  static_assert(lim(12) <= 50, "vD / vC must be ready before the first phase-2 unit");

  Y(IC<7>{}, 0, true);
  {
    const int l = lane_id(), r32 = l & 31;
    f_a = ldsf4(M_SP + (32 * mw + r32) * 16);
    f_b = ldsf4(M_CSB + (32 * nw + r32) * 16);
  }
  rdW(0, 22, 0);
  issue_nth(IC<7>{}, IC<0>{});
  sfor<0, 2>([&](auto Pc) AI {
    constexpr int P = decltype(Pc)::value;
    constexpr int S0 = 7 + 7 * P, U0 = 22 + 14 * P;
    f32x16 acc0 = {0}, acc1 = {0};
    float cs0 = 0.f, cs1 = 0.f;
    sfor<0, 7>([&](auto kbc) AI {
      constexpr int kb = decltype(kbc)::value;
      constexpr int S = S0 + kb, ug = U0 + 2 * kb;
      u32x4 c0, c1;
      sfor<0, 4>([&](auto kslc) AI {
        constexpr int ksl = decltype(kslc)::value, Q = 4 * (S - 7) + ksl;
        const u32x4 xv = xf[4 * kb + ksl];
        SB();
        ST4(Q);
        acc0 = mfma32(wf[ksl & 1][0], xv, acc0);
        PIN(acc0);
        SB();
        if constexpr (ksl < 3) rdW((ksl + 1) & 1, ug, (ksl + 1) * 2048);
        else if constexpr (S < 20) { const u32x4 keep = wf[1][1]; rdW(0, ug + 2, 0); wf[1][1] = keep; }
        else rdB(IC<0>{});
        if constexpr (ksl == 0) {
          const u32 o = (ug & 7) * 8192;
          const u32 b0 = ((ug & 8) ? aW0h : aW0) + mw * 2048, b1 = ((ug & 8) ? aW1h : aW1) + mw * 2048;
          c0 = ldstr2(b0 + o, b0 + o + 512);
          c1 = ldstr2(b1 + o, b1 + o + 512);
        }
        if constexpr (ksl == 3 && kb == 6) {
          const int l = lane_id(), r32 = l & 31;
          f_a = ldsf4(M_CS + ((nw * 2 + 0) * 32 + r32) * 16);
          f_b = ldsf4(M_CS + ((nw * 2 + 1) * 32 + r32) * 16);
        }
        SB();
        acc1 = mfma32(wf[ksl & 1][1], xv, acc1);
        PIN(acc1);
        SB();
        if constexpr (ksl == 1) {
          cs0 = dot2(c0.x, ONES, cs0); cs1 = dot2(c1.x, ONES, cs1); cs0 = dot2(c0.y, ONES, cs0); cs1 = dot2(c1.y, ONES, cs1);
          cs0 = dot2(c0.z, ONES, cs0); cs1 = dot2(c1.z, ONES, cs1); cs0 = dot2(c0.w, ONES, cs0); cs1 = dot2(c1.w, ONES, cs1);
          PIN(cs0); PIN(cs1);
          if constexpr (kb == 6) {
            const int l = lane_id(), r32 = l & 31;
            stsf(M_CS + ((nw * 2 + 0) * 32 + r32) * 16 + mw * 4, xhalf_sum(cs0));
            stsf(M_CS + ((nw * 2 + 1) * 32 + r32) * 16 + mw * 4, xhalf_sum(cs1));
          }
        }
        fill(IC<Q>{});
        if constexpr (ksl == 3 && kb == 6) {
          f_t0 = (f_a.x + f_a.y) + (f_a.z + f_a.w); f_t1 = (f_b.x + f_b.y) + (f_b.z + f_b.w);
        }
        SB();
        if constexpr (ksl == 3) issue_nth(IC<S + 1>{}, IC<0>{});
        if constexpr (ksl == 0) issue_nth(IC<S>{}, IC<2>{});
        if constexpr (ksl == 1) issue_nth(IC<S>{}, IC<1>{});
        if constexpr (Q >= 51) issueA(IC<Q - 51>{});
        if constexpr (ksl == 2) {
          int xp = 0;
          if (Q == 22) xp = pos_gran;
          if (Q == 34) xp = pos_ha;
          if (Q == 46) xp = pos_hs;
          if constexpr (Q == 50) poll_check(0);
          Y(IC<S + 1>{}, xp, Q == 10 || Q == 26 || kb == 6);
          if constexpr (Q == 34) flag_store(0);
          if constexpr (Q == 46) flag_store(1);
        }
      });
    });
    ST4(56 + 2 * P);
    {
      const u32x4 xm = u32x4{xm0, xm1, 0u, 0u};
      acc0 = mfma32(split3(f_t0), xm, acc0);
      acc1 = mfma32(split3(f_t1), xm, acc1);
    }
    {
      u32x4 o0, o1;
      const int ks = 8 * j + 4 * P + 2 * nw;
      const u32 vL16 = aT & 1023u;
#pragma unroll
      for (int i = 0; i < 4; ++i) o0[i] = pack2(fmaxf(acc0[2 * i], 0.f) * acc0[2 * i + 8], fmaxf(acc0[2 * i + 1], 0.f) * acc0[2 * i + 9]);
      gst16_sc1(bH + ks * 1024, vL16, o0);
#pragma unroll
      for (int i = 0; i < 4; ++i) o1[i] = pack2(fmaxf(acc1[2 * i], 0.f) * acc1[2 * i + 8], fmaxf(acc1[2 * i + 1], 0.f) * acc1[2 * i + 9]);
      gst16_sc1(bH + ks * 1024 + 1024, vL16, o1);
      vm += 2;
      if constexpr (P == 0) pos_ha = vm; else pos_hb = vm;
    }
    ST4(57 + 2 * P);
    if constexpr (P == 1) { issue_nth(IC<21>{}, IC<1>{}); issue_nth(IC<21>{}, IC<2>{}); sfor<5, AWIN>(issueA); }
  });

  f32x4 af[4], as[4];
#pragma unroll
  for (int i = 0; i < 4; ++i) { af[i] = f32x4{0, 0, 0, 0}; as[i] = f32x4{0, 0, 0, 0}; }
  u32x2 xr[2], dsk[2];
  float e_x[8], e_sk[8], e_rs2, e_mb, e_rs;
  int tok, col0;
  sfor<0, 42>([&](auto tc) AI {
    constexpr int t = decltype(tc)::value, T = 22 + (t & ~1);
    if constexpr ((t & 1) == 0) {
      int xp = posA[t];
      if (t == 4 && pos_hb > xp) xp = pos_hb;
      if constexpr (t == ((13 - AWIN) & ~1)) poll_check(1);
      if constexpr (t == ((27 - AWIN) & ~1)) poll_check(2);
      Y(IC<T>{}, xp, false);
      if constexpr (t == 4) flag_store(2);
    } else {
      SB(); wait_vm(vm - posA[t]); SB();
    }
    issueA(IC<t + AWIN>{});
    if constexpr (t == 0) poll_issue(1);
    if constexpr (t == 20 - AWIN) poll_issue(2);
    SB();
    if constexpr (t + 1 < 42) rdB(IC<t + 1>{});
    SB();
    asm volatile("" : "+v"(Aw[t % AB]));
    const u32x4 av = Aw[t % AB];
    if constexpr (t >= 14 && t < 28) {
#pragma unroll
      for (int n = 0; n < 4; ++n) { as[n] = mfma16(bfr[t & 1][n], av, as[n]); PIN(as[n]); }
    } else {
#pragma unroll
      for (int n = 0; n < 4; ++n) { af[n] = mfma16(bfr[t & 1][n], av, af[n]); PIN(af[n]); }
    }
    if constexpr (t == 32 - AWIN) {
      const int l = lane_id(), q = l >> 4, s16 = l & 15;
      tok = 64 * mb2 + 32 * (mq >> 1) + 16 * ((s16 >> 2) & 1) + (s16 & 3) + 8 * (mq & 1) + 4 * (s16 >> 3);
      col0 = 64 * nb + 32 * kq + 4 * q;
#pragma unroll
      for (int n = 0; n < 2; ++n) {
        gld8(xr[n], bX, (u32)(tok * ROWB + (col0 + 16 * n) * 2));
        gld8(dsk[n], p.Dsk, (u32)((col0 + 16 * n) * 2));
      }
      vm += 4;
    }
    if constexpr (t == 33) {
      asm volatile("" : "+v"(xr[0]), "+v"(xr[1]), "+v"(dsk[0]), "+v"(dsk[1]));
      e_mb = *(const LAS float*)(uintptr_t)(M_STAT + tok * 4); e_rs = *(const LAS float*)(uintptr_t)(M_STAT + 512 + tok * 4);
      e_rs2 = e_rs * e_rs;
      PIN(e_mb); PIN(e_rs); PIN(e_rs2);
    }
    if constexpr (t >= 34) {
      constexpr int i = t - 34;
      const u32 xw = xr[i >> 2][(i >> 1) & 1], dw = dsk[i >> 2][(i >> 1) & 1];
      const float xv = (i & 1) ? bfhi(xw) : bf2f(xw), dv = (i & 1) ? bfhi(dw) : bf2f(dw);
      const float xn = rbf(rbf(xv - e_mb) * e_rs);
      e_x[i] = xv; e_sk[i] = rbf(xn * dv);
      PIN(e_x[i]); PIN(e_sk[i]);
    }
    SB();
    if constexpr ((t & 1) == 0) { issue_nth(IC<T>{}, IC<0>{}); } else { issue_nth(IC<T>{}, IC<1>{}); }
  });
  asm volatile("s_waitcnt vmcnt(0) lgkmcnt(0)\n\ts_barrier" ::: "memory");
  {
    const int l = lane_id();
    const u32 xb = (mq * 2 + (kq ^ 1)) * 4096 + l * 16, rb = (mq * 2 + kq) * 4096 + l * 16;
    const f32x4 sf0 = kq ? af[0] : af[2], sf1 = kq ? af[1] : af[3], ss0 = kq ? as[0] : as[2], ss1 = kq ? as[1] : as[3];
    *(LAS f32x4*)(uintptr_t)(xb) = sf0; *(LAS f32x4*)(uintptr_t)(xb + 1024) = sf1;
    *(LAS f32x4*)(uintptr_t)(xb + 2048) = ss0; *(LAS f32x4*)(uintptr_t)(xb + 3072) = ss1;
    asm volatile("s_waitcnt lgkmcnt(0)\n\ts_barrier" ::: "memory");
    f32x4 kf[2] = {kq ? af[2] : af[0], kq ? af[3] : af[1]}, ks[2] = {kq ? as[2] : as[0], kq ? as[3] : as[1]};
    kf[0] += ldsf4(rb); kf[1] += ldsf4(rb + 1024); ks[0] += ldsf4(rb + 2048); ks[1] += ldsf4(rb + 3072);
#pragma unroll
    for (int n = 0; n < 2; ++n) {
      f32x4 o;
#pragma unroll
      for (int i = 0; i < 4; ++i) {
        const float y = ks[n][i] + e_sk[4 * n + i];
        const float f = rbf(kf[n][i] * e_rs2);
        const float gt = rbf(f * __builtin_amdgcn_rcpf(1.f + __builtin_amdgcn_exp2f(-1.44269504f * f)));
        o[i] = __builtin_fmaf(y, gt, e_x[4 * n + i]);
      }
      *(f32x4*)(p.out + (size_t)(128 * g + tok) * 448 + col0 + 16 * n) = o;
    }
  }
  if (g == 15 && j == 0 && threadIdx.x == 0) *(volatile u32*)p.ws = tag;
  asm volatile(".Lpf_end:" ::: "memory");
}

extern "C" void kernel_launch(void* const* d_in, const int* in_sizes, int n_in, void* d_out, int out_size, void* d_ws,
                              size_t ws_size, hipStream_t stream) {
  Params p;
  p.x = (const u16*)d_in[0]; p.lre = (const u16*)d_in[1]; p.lim = (const u16*)d_in[2];
  p.Bre = (const u16*)d_in[3]; p.Bim = (const u16*)d_in[4]; p.Cre = (const u16*)d_in[5]; p.Cim = (const u16*)d_in[6];
  p.ldt = (const u16*)d_in[7]; p.Dsk = (const u16*)d_in[8];
  p.Wg = (const u16*)d_in[10]; p.Wu = (const u16*)d_in[11]; p.Wd = (const u16*)d_in[12];
  p.out = (float*)d_out; p.ws = (char*)d_ws;
  (void)hipFuncSetAttribute((const void*)fused_ssm_ffn, hipFuncAttributeMaxDynamicSharedMemorySize, LDS_BYTES);
  fused_ssm_ffn<<<224 + NHELP, 512, LDS_BYTES, stream>>>(p);
}
```

```cpp
#include <hip/hip_runtime.h>
#include <stdint.h>

typedef unsigned short u16;
typedef unsigned u32;
typedef unsigned long long u64;
typedef __attribute__((ext_vector_type(8))) __bf16 bf16x8;
typedef __attribute__((ext_vector_type(2))) __bf16 bf16x2;
typedef __attribute__((ext_vector_type(4))) short s16x4;
typedef __attribute__((ext_vector_type(16))) float f32x16;
typedef __attribute__((ext_vector_type(4))) float f32x4;
typedef __attribute__((ext_vector_type(2))) float f32x2;
typedef __attribute__((ext_vector_type(4))) u32 u32x4;
typedef __attribute__((ext_vector_type(2))) u32 u32x2;
#define LAS __attribute__((address_space(3)))
#if defined(__HIP_DEVICE_COMPILE__)
#define NOPK __attribute__((target("no-packed-fp32-ops")))
#else
#define NOPK
#endif
#define FI __device__ __forceinline__ NOPK

#define NHELP 32
#define OUTS "sc1 nt"
#define PRE_ST 5
#define PB 2
#define TLAST 32
#define RAMP 5
#define AWIN 6

constexpr int ROWB = 896;
constexpr int ROWT = 1792;
constexpr int ROWW = 3584;
constexpr int NKS = 168;
constexpr u32 M_TRANS = 131072, M_CONST = 147456, M_STAT = 159744,
              M_W = 160768, M_H = 161792, M_DUMMY = 163072, LDS_BYTES = 163840;
constexpr size_t WS_FLAG = 4096, WS_GRAN = 65536, WS_PRE = 262144, WS_H = 1u << 20, WS_WDT = 16u << 20;
constexpr u32 PRE_STAT = 0, PRE_CSB = 32768, PRE_CSW = 40960, PRE_LAM = 73728, PRE_TF = 98304;
constexpr int NUNIT = 92, RING = 16;
constexpr int imin(int a, int b) { return a < b ? a : b; }
constexpr int cum(int S) { return S < 0 ? 0 : S < 6 ? 3 * (S + 1) : S < 14 ? 22 + 2 * (S - 6) : S < 21 ? 36 + 2 * (S - 13) : 50 + (S - 20); }
constexpr int lim(int T) { return T < 0 ? 6 : T < 22 ? imin(cum(T - 2) + RING, lim(T - 1) + (T < 2 ? RAMP : 3)) : imin(NUNIT, 66 + PB * ((T - 22) / PB)); }

struct Params {
  const u16 *x, *lre, *lim, *Bre, *Bim, *Cre, *Cim, *ldt, *Dsk, *Wg, *Wu, *Wd;
  float* out;
  char* ws;
};

FI float u2f(u32 v) { return __builtin_bit_cast(float, v); }
FI u32 f2u(float v) { return __builtin_bit_cast(u32, v); }
FI void wait_vm(int n) {
  switch (n < 63 ? n : 63) {
#define C1(i) case i: asm volatile("s_waitcnt vmcnt(" #i ")" ::: "memory"); break;
    C1(0) C1(1) C1(2) C1(3) C1(4) C1(5) C1(6) C1(7) C1(8) C1(9) C1(10) C1(11) C1(12) C1(13) C1(14) C1(15)
    C1(16) C1(17) C1(18) C1(19) C1(20) C1(21) C1(22) C1(23) C1(24) C1(25) C1(26) C1(27) C1(28) C1(29) C1(30) C1(31)
    C1(32) C1(33) C1(34) C1(35) C1(36) C1(37) C1(38) C1(39) C1(40) C1(41) C1(42) C1(43) C1(44) C1(45) C1(46) C1(47)
    C1(48) C1(49) C1(50) C1(51) C1(52) C1(53) C1(54) C1(55) C1(56) C1(57) C1(58) C1(59) C1(60) C1(61) C1(62) C1(63)
#undef C1
  }
}
FI void dma16(const char* base, u32 voff, u32 ldsaddr, bool skip = false) {
  if (skip) return;
  asm volatile("s_mov_b32 m0, %2\n\ts_nop 0\n\tglobal_load_lds_dwordx4 %0, %1" ::"v"(voff), "s"(base), "s"(ldsaddr) : "memory");
}
FI void dma16_sc1(const char* base, u32 voff, u32 ldsaddr) {
  asm volatile("s_mov_b32 m0, %2\n\ts_nop 0\n\tglobal_load_lds_dwordx4 %0, %1 sc1" ::"v"(voff), "s"(base), "s"(ldsaddr) : "memory");
}
FI void touch(const char* base, u32 voff) {
  asm volatile("s_mov_b32 m0, %2\n\ts_nop 0\n\tglobal_load_lds_dword %0, %1" ::"v"(voff), "s"(base), "s"(M_DUMMY) : "memory");
}
FI void gld16_sc1(u32x4& d, const char* base, u32 voff) {
  asm volatile("global_load_dwordx4 %0, %1, %2 sc1" : "=v"(d) : "v"(voff), "s"(base) : "memory");
}
FI void gst16_sc1(const char* base, u32 voff, u32x4 d) {
  asm volatile("global_store_dwordx4 %0, %1, %2 sc1\n\ts_nop 1" ::"v"(voff), "v"(d), "s"(base) : "memory");
}
FI void regld16(u32x4& d, const char* base, u32 voff) {
  asm volatile("global_load_dwordx4 %0, %1, %2 sc1\n\ts_waitcnt vmcnt(0)" : "+v"(d) : "v"(voff), "s"(base) : "memory");
}
FI void regld8(u32x2& d, const char* base, u32 voff) {
  asm volatile("global_load_dwordx2 %0, %1, %2 sc1\n\ts_waitcnt vmcnt(0)" : "+v"(d) : "v"(voff), "s"(base) : "memory");
}
FI u32 tagbad(u32x4 v, u32 tag) { return (v.y ^ tag) | (v.w ^ tag); }
constexpr int SPIN_MAX = 1 << 20;
FI void gst8_sc1(const char* base, u32 voff, u32x2 d) {
  asm volatile("global_store_dwordx2 %0, %1, %2 sc1" ::"v"(voff), "v"(d), "s"(base) : "memory");
}
FI void gld8_sc1(u32x2& d, const char* base, u32 voff) {
  asm volatile("global_load_dwordx2 %0, %1, %2 sc1" : "=v"(d) : "v"(voff), "s"(base) : "memory");
}
FI void gld8(u32x2& d, const void* base, u32 voff) {
  asm volatile("global_load_dwordx2 %0, %1, %2" : "=v"(d) : "v"(voff), "s"(base) : "memory");
}
FI u32x4 lds16(u32 a) { return *(const LAS u32x4*)(uintptr_t)a; }
FI f32x4 ldsf4(u32 a) { return *(const LAS f32x4*)(uintptr_t)a; }
FI f32x2 ldsf2(u32 a) { return *(const LAS f32x2*)(uintptr_t)a; }
FI void stsf(u32 a, float v) { *(LAS float*)(uintptr_t)a = v; }
FI void stsf2(u32 a, float x, float y) { *(LAS f32x2*)(uintptr_t)a = f32x2{x, y}; }
FI u32x2 ldstr(u32 a) {
  s16x4 v = __builtin_amdgcn_ds_read_tr16_b64_v4i16((LAS s16x4*)(uintptr_t)a);
  return __builtin_bit_cast(u32x2, v);
}
FI u32x4 ldstr2(u32 a0, u32 a1) {
  u32x2 lo = ldstr(a0), hi = ldstr(a1);
  return u32x4{lo.x, lo.y, hi.x, hi.y};
}
FI f32x16 mfma32(u32x4 a, u32x4 b, f32x16 c) {
  return __builtin_amdgcn_mfma_f32_32x32x16_bf16(__builtin_bit_cast(bf16x8, a), __builtin_bit_cast(bf16x8, b), c, 0, 0, 0);
}
FI f32x4 mfma16(u32x4 a, u32x4 b, f32x4 c) {
  return __builtin_amdgcn_mfma_f32_16x16x32_bf16(__builtin_bit_cast(bf16x8, a), __builtin_bit_cast(bf16x8, b), c, 0, 0, 0);
}
FI float dot2(u32 a, u32 b, float c) {
  return __builtin_amdgcn_fdot2_f32_bf16(__builtin_bit_cast(bf16x2, a), __builtin_bit_cast(bf16x2, b), c, false);
}
FI u32 pack2(float a, float b) {
  bf16x2 v = {(__bf16)a, (__bf16)b};
  return __builtin_bit_cast(u32, v);
}
FI float rbf(float a) { return u2f(pack2(a, a) << 16); }
FI float bf2f(u32 v) { return u2f(v << 16); }
FI float bfhi(u32 v) { return u2f(v & 0xffff0000u); }
FI void swap16(float v, float& even, float& odd) {
  auto r = __builtin_amdgcn_permlane16_swap(f2u(v), f2u(v), false, false);
  even = u2f(r[0]); odd = u2f(r[1]);
}
FI void swap32(float v, float& lo, float& hi) {
  auto r = __builtin_amdgcn_permlane32_swap(f2u(v), f2u(v), false, false);
  lo = u2f(r[0]); hi = u2f(r[1]);
}
FI float xhalf_sum(float v) { float a, b; swap32(v, a, b); return a + b; }
FI bool wany(bool c) { return __builtin_amdgcn_ballot_w64(c) != 0; }
struct cf { float r, i; };
FI cf cmul(cf a, cf b) { return cf{a.r * b.r - a.i * b.i, a.r * b.i + a.i * b.r}; }
FI cf csqr(cf a) { return cf{a.r * a.r - a.i * a.i, (a.r + a.r) * a.i}; }
FI cf cfma(cf a, cf b, cf c) {
  return cf{__builtin_fmaf(-a.i, b.i, __builtin_fmaf(a.r, b.r, c.r)), __builtin_fmaf(a.i, b.r, __builtin_fmaf(a.r, b.i, c.i))};
}
FI cf csel(bool p, cf a, cf b) { return cf{p ? a.r : b.r, p ? a.i : b.i}; }
FI float exp_c(float x) {
  const float t = x * 1.44269502162933349609f;
  const float r = __builtin_fmaf(x, 1.92596299112661746e-8f, __builtin_fmaf(x, 1.44269502162933349609f, -t));
  const float y = __builtin_amdgcn_exp2f(t);
  return __builtin_fmaf(y, r * 0.693147182464599609375f, y);
}
FI u32x4 split3(float cs) {
  const float hi = rbf(cs), r1 = cs - hi, mid = rbf(r1), lo = r1 - mid;
  return u32x4{pack2(hi, mid), pack2(lo, 0.f), 0u, 0u};
}
#define SB() __builtin_amdgcn_sched_barrier(0)
FI int lane_id() { int v; asm volatile("v_mbcnt_lo_u32_b32 %0, -1, 0\n\tv_mbcnt_hi_u32_b32 %0, -1, %0" : "=v"(v)); return v; }
template <int V> struct IC { static constexpr int value = V; };
template <int I, int N, class F> FI void sfor(F&& f) {
  if constexpr (I < N) { f(IC<I>{}); sfor<I + 1, N>(f); }
}
#define AI __attribute__((always_inline)) NOPK
#define PIN(x) asm volatile("" : "+v"(x))
#define PINC(c) asm volatile("" : "+v"((c).r), "+v"((c).i))

__global__ __launch_bounds__(512, 2) NOPK void fused_ssm_ffn(Params p) {
  const int w = __builtin_amdgcn_readfirstlane(threadIdx.x >> 6);
  const int mw = w & 3, nw = w >> 2, mq = mw, kq = nw;
  if (blockIdx.x < NHELP) {
    const int hb = blockIdx.x, hw = hb * 8 + w, l = lane_id();

    const int n0 = ((hb >> 3) * 8 + w) * 64 + l;
    u32 ep;
    asm volatile("global_load_dword %0, %1, %2 sc1" : "=v"(ep) : "v"(0u), "s"(p.ws) : "memory");
    u64 pc0;
    u32 len;
    int back;
    asm volatile("s_getpc_b64 %0\n.Lpf_h:\n\ts_mov_b32 %1, .Lpf_end-.Lpf_begin\n\ts_mov_b32 %2, .Lpf_h-.Lpf_begin" : "=s"(pc0), "=s"(len), "=s"(back));
    if ((u32)n0 * 64 + 4 <= len) touch((const char*)(pc0 - (long long)back), (u32)n0 * 64);
    const char* pre = p.ws + WS_PRE;
    if (hw >= 112 && hw < 119) {
      const int st = (hw - 112) * 64 + l;
      const float k_lr = bf2f(p.lre[st]), k_li = bf2f(p.lim[st]);
      const float dt = rbf(exp_c(bf2f(p.ldt[st])));
      const float k_e = exp_c(dt * k_lr), k_th = dt * k_li;
      float k_s, k_co;
      if (__builtin_expect(wany(!(__builtin_fabsf(k_th) <= 4096.f)), 0)) {
        k_s = sinf(k_th); k_co = cosf(k_th);
      } else {
        const float k = __builtin_rintf(k_th * 0.636619746685028076171875f);
        float r = __builtin_fmaf(-k, 1.57079637050628662109375f, k_th);
        r = __builtin_fmaf(-k, -4.37113882867379118e-8f, r);
        const float z = r * r;
        float ps = __builtin_fmaf(z, -1.9515295891e-4f, 8.3321608736e-3f);
        ps = __builtin_fmaf(z, ps, -1.6666654611e-1f);
        const float sn = __builtin_fmaf(r * z, ps, r);
        float pc = __builtin_fmaf(z, 2.443315711809948e-5f, -1.388731625493765e-3f);
        pc = __builtin_fmaf(z, pc, 4.166664568298827e-2f);
        const float cs = __builtin_fmaf(z * z, pc, __builtin_fmaf(z, -0.5f, 1.f));
        const int ki = (int)k;
        const float s1 = (ki & 1) ? cs : sn, c1 = (ki & 1) ? sn : cs;
        k_s = u2f(f2u(s1) ^ ((u32)(ki & 2) << 30));
        k_co = u2f(f2u(c1) ^ ((u32)((ki + 1) & 2) << 30));
      }
      const float lr = k_e * k_co, li = k_e * k_s;
      const float nr = lr - 1.f, dd = k_lr * k_lr + k_li * k_li;
      float rc = __builtin_amdgcn_rcpf(dd);
      rc = __builtin_fmaf(__builtin_fmaf(-dd, rc, 1.f), rc, rc);
      const float d2 = rc + rc;
      asm volatile("s_waitcnt vmcnt(0)" : "+v"(ep) :: "memory");
      const u32 t2 = ep + 1;
      gst16_sc1(pre + PRE_LAM, (u32)st * 32, u32x4{f2u(lr), t2, f2u(li), t2});
      gst16_sc1(pre + PRE_LAM, (u32)st * 32 + 16, u32x4{f2u((nr * k_lr + li * k_li) * d2), t2, f2u((li * k_lr - nr * k_li) * d2), t2});
    }
    const int rr = 8 * hw + (l >> 3);
    u32x4 xv[7], bv[7];
    const char* px = (const char*)p.x + rr * ROWB + (l & 7) * 16;
#pragma unroll
    for (int i = 0; i < 7; ++i) xv[i] = *(const u32x4*)(px + 128 * i);
    const bool dob = hw < 112;
    const char* pb = (const char*)(hw < 56 ? p.Bre : p.Bim) + (dob ? (hw < 56 ? rr : rr - 448) * ROWB + (l & 7) * 16 : 0);
#pragma unroll
    for (int i = 0; i < 7; ++i) bv[i] = *(const u32x4*)(pb + 128 * i);
    auto red8 = [&](float v) AI {
      v += u2f(__builtin_amdgcn_ds_swizzle(f2u(v), 0x041F));
      v += u2f(__builtin_amdgcn_ds_swizzle(f2u(v), 0x081F));
      v += u2f(__builtin_amdgcn_ds_swizzle(f2u(v), 0x101F));
      return v;
    };
    const u32 ONE2 = 0x3f803f80u;
    float sx = 0.f;
#pragma unroll
    for (int i = 0; i < 7; ++i) { sx = dot2(xv[i].x, ONE2, sx); sx = dot2(xv[i].y, ONE2, sx); sx = dot2(xv[i].z, ONE2, sx); sx = dot2(xv[i].w, ONE2, sx); }
    const float mean = red8(sx) * (1.f / 448.f);
    float q0 = 0.f, q1 = 0.f;
#pragma unroll
    for (int i = 0; i < 7; ++i) {
#pragma unroll
      for (int k = 0; k < 4; ++k) {
        const float d0 = bf2f(xv[i][k]) - mean, d1 = bfhi(xv[i][k]) - mean;
        q0 = __builtin_fmaf(d0, d0, q0); q1 = __builtin_fmaf(d1, d1, q1);
      }
    }
    const float var = rbf(red8(q0 + q1) * (1.f / 448.f));
    const float den = rbf(__builtin_amdgcn_sqrtf(rbf(var + 1e-5f)));
    asm volatile("" : "+v"(ep) : "v"(den));
    const u32 htag = ep + 1;
    if ((l & 7) == 0) gst16_sc1(pre + PRE_STAT, (u32)rr * 16, u32x4{f2u(rbf(mean)), htag, f2u(__builtin_amdgcn_rcpf(den)), htag});
    if (dob) {
      float sb = 0.f;
#pragma unroll
      for (int i = 0; i < 7; ++i) { sb = dot2(bv[i].x, ONE2, sb); sb = dot2(bv[i].y, ONE2, sb); sb = dot2(bv[i].z, ONE2, sb); sb = dot2(bv[i].w, ONE2, sb); }
      sb = red8(sb);
      if ((l & 7) == 0) gst8_sc1(pre + PRE_CSB, (u32)rr * 8, u32x2{f2u(sb), htag});
    }

    if (hb < 28) {
      const int img = hb >= 14, jj = hb - 14 * img;
      const char* pw = (const char*)(img ? p.Wu : p.Wg) + (56 * w + (l >> 4)) * ROWW + jj * 256 + (l & 15) * 16;
      u32x4 wv[14];
#pragma unroll
      for (int i = 0; i < 14; ++i) wv[i] = *(const u32x4*)(pw + 4 * i * ROWW);
      float cs[8];
      u32 sel0 = 0x00003f80u, sel1 = 0x3f800000u;
      asm volatile("" : "+v"(sel0), "+v"(sel1));
#pragma unroll
      for (int k = 0; k < 8; ++k) cs[k] = 0.f;
#pragma unroll
      for (int i = 0; i < 14; ++i) {
#pragma unroll
        for (int k = 0; k < 4; ++k) { cs[2 * k] = dot2(wv[i][k], sel0, cs[2 * k]); cs[2 * k + 1] = dot2(wv[i][k], sel1, cs[2 * k + 1]); }
      }
#pragma unroll
      for (int k = 0; k < 8; ++k) {
        float a0, a1;
        swap16(cs[k], a0, a1); cs[k] = a0 + a1;
        cs[k] = xhalf_sum(cs[k]);
      }
      if (l < 16) {
        *(LAS f32x4*)(uintptr_t)(w * 512 + l * 32) = f32x4{cs[0], cs[1], cs[2], cs[3]};
        *(LAS f32x4*)(uintptr_t)(w * 512 + l * 32 + 16) = f32x4{cs[4], cs[5], cs[6], cs[7]};
      }
      asm volatile("s_waitcnt lgkmcnt(0)\n\ts_barrier" ::: "memory");
      if (threadIdx.x < 128) {
        const int c = threadIdx.x;
        float t = 0.f;
#pragma unroll
        for (int k = 0; k < 8; ++k) t += *(const LAS float*)(uintptr_t)(k * 512 + c * 4);
        gst8_sc1(pre + PRE_CSW, (u32)((((jj * 2 + (c >> 6)) * 2 + ((c >> 5) & 1)) * 32 + img * 16 + (c & 15)) * 16 + ((c >> 4) & 1) * 8), u32x2{f2u(t), htag});
      }
    }

    const char* wdt = p.ws + WS_WDT;
    {
      const u32 m0 = hb * 512 + threadIdx.x;
      if (m0 < 6272) { const u32 row = m0 / 7; touch((const char*)p.Wd, (row + (row & ~63u)) * ROWB + (m0 - row * 7) * 128); }
      else if (m0 < 9408) touch((const char*)p.Cre, (m0 - 6272) * 128);
      else if (m0 < 12544) touch((const char*)p.Cim, (m0 - 9408) * 128);
    }
    if (hb >= 18) {
      constexpr u32 PITCH = 928, TB = 8192;
      const int tile = hb - 18;
      u32x4 dv[7];
#pragma unroll
      for (int i = 0; i < 7; ++i) {
        const u32 c = threadIdx.x + 512 * i, row = c / 56;
        dv[i] = *(const u32x4*)((const char*)p.Wd + (128 * tile + 64 + row) * ROWB + (c - row * 56) * 16);
      }
#pragma unroll
      for (int i = 0; i < 7; ++i) {
        const u32 c = threadIdx.x + 512 * i, row = c / 56;
        *(LAS u32x4*)(uintptr_t)(TB + row * PITCH + (c - row * 56) * 16) = dv[i];
      }
      asm volatile("s_waitcnt lgkmcnt(0)\n\ts_barrier" ::: "memory");

      const u32 q = l >> 4, s16 = l & 15;
      const u32 ra = TB + (16 * (q >> 1) + 4 * (q & 1) + (s16 >> 2)) * PITCH + (l & 3) * 8;
#pragma unroll
      for (int i = 0; i < 7; ++i) {
        const u32 it = 7 * w + i, gp = it & 1, np = it >> 1;
        const u32 a = ra + gp * 32 * PITCH + np * 32;
        gst16_sc1(wdt, (16 * np + s16) * ROWT + tile * 128 + (4 * gp + q) * 16, ldstr2(a, a + 8 * PITCH));
      }

      asm volatile("s_waitcnt vmcnt(0)\n\ts_barrier" ::: "memory");

      if (threadIdx.x == 0) gst8_sc1(pre + PRE_TF, (u32)tile * 64, u32x2{htag, htag});
    }
#pragma unroll 1
    for (int r = 0; r < 10; ++r) {
      const u32 n = r * 2048 + n0;
      if (r == 6) {
        u32x2 fv = {0u, 0u};
        int spins = 0;
        do { regld8(fv, pre + PRE_TF, (u32)(l < 14 ? l : 0) * 64); } while (wany(((fv.x ^ htag) | (fv.y ^ htag)) != 0) && ++spins < SPIN_MAX);
      }
      if (n < 6272) { const u32 row = n / 7; touch((const char*)p.Wd, (row + (row & ~63u)) * ROWB + (n - row * 7) * 128); }
      else if (n < 12288) {
        const u32 kb = (n - 6272) / 448, d = n - 6272 - kb * 448;
        if (kb & 1) touch((const char*)p.Cim, d * ROWB + (kb >> 1) * 128); else touch((const char*)p.Cre, d * ROWB + (kb >> 1) * 128);
      } else if (n < 12544) touch((const char*)p.Cim, (n - 12288 + 192) * ROWB + 6 * 128);
      else if (n < 18816) { const u32 kb = (n - 12544) / 448, d = n - 12544 - kb * 448; touch(wdt, d * ROWT + kb * 128); }
    }

    asm volatile("s_waitcnt vmcnt(0)" ::: "memory");
    return;
  }
  const int b = blockIdx.x - NHELP, xcd = b & 7, slot = b >> 3;
  const int g = 4 * (xcd >> 1) + slot / 7, j = 7 * (xcd & 1) + slot % 7;

  int vm = 0;
  int pos[NUNIT];

  u64 pcb;
  asm volatile("s_getpc_b64 %0\n.Lpf_begin:" : "=s"(pcb));
  u32 v_ep;
  u32 vX, vB;
  const int lane0 = lane_id();
  {
    const int l = lane0;
    const int dr = 8 * w + (l >> 3), dc = l & 7;
    const int dr31 = dr & 31;
    const u32 sw = (dc ^ ((dr >> 1) & 7)) << 4;
    vX = (u32)((32 * (dr >> 5) + 16 * ((dr31 >> 2) & 1) + (dr31 & 3) + 4 * (dr31 >> 3)) * ROWB) + sw;
    vB = (u32)((16 * (w >> 2) + 8 * (w & 1) + (l >> 3)) * ROWB) + sw;
    PIN(vX); PIN(vB);
    SB();
  }
  u32 vG, vU, vD, vC, vT;
  auto make_vGU = [&]() AI {
    const int l = lane_id();
    const int dr = 8 * w + (l >> 3), dc = l & 7;
    vG = (u32)(dr * ROWW + ((dc ^ (((dr >> 1) & 1) << 1)) << 4));
    vU = vG ^ 64u;
  };
  auto make_vDC = [&]() AI {
    const int l = lane_id();
    const int dr = 8 * w + (l >> 3), dc = l & 7;
    vD = (u32)(dr * ROWB + ((dc ^ (((dr >> 1) & 3) << 1)) << 4));
    vT = (u32)(dr * ROWT + ((dc ^ ((dr >> 1) & 7)) << 4));
    vC = (u32)(dr * ROWB + ((dc ^ ((dr >> 1) & 7)) << 4));
  };
  const char* bX = (const char*)p.x + (size_t)g * 128 * ROWB;
  const char* bB = (const char*)(((w >> 1) & 1) ? p.Bim : p.Bre) + (size_t)j * 32 * ROWB;
  const char* bG = (const char*)p.Wg + j * 256;
  const char* bU = (const char*)p.Wu + j * 256;
  const int nb = j % 7, mb2 = j / 7;
  const char* bD = (const char*)p.Wd + nb * 128;
  const char* bT = p.ws + WS_WDT + (size_t)nb * 64 * ROWT;
  const char* bCr = (const char*)p.Cre + (size_t)nb * 64 * ROWB;
  const char* bCi = (const char*)p.Cim + (size_t)nb * 64 * ROWB;
  const u32 ldsw = w * 1024;

  auto issue_unit = [&](auto uc) AI {
    constexpr int u = decltype(uc)::value;
    const u32 dst = ldsw + (u & 15) * 8192;
    if constexpr (u < 21) {
      constexpr int S = u / 3, rr = u % 3;
      constexpr int kind = (S & 1) ? (rr == 0 ? 2 : rr - 1) : rr;
      if constexpr (kind == 2) dma16(bB + S * 128, vB, dst);
      else dma16(bX + kind * 64 * ROWB + S * 128, vX, dst);
    } else if constexpr (u == 21) {
      return;
    } else if constexpr (u < 50) {
      constexpr int P = (u - 22) / 14, kb = ((u - 22) % 14) >> 1, img = u & 1;
      dma16((img ? bU : bG) + (size_t)kb * 64 * ROWW + P * 128, img ? vU : vG, dst);
    } else {
      constexpr int t = u - 50;
      if constexpr (t < 14) dma16(bD + (size_t)(128 * t) * ROWB, vD, dst);
      else if constexpr (t < 28) dma16((((t - 14) & 1) ? bCi : bCr) + ((t - 14) >> 1) * 128, vC, dst);
      else dma16_sc1(bT + 128 * (t - 28), vT, dst);
    }
    pos[u] = ++vm;
  };
  auto issue_nth = [&](auto Tc, auto nc) AI {
    constexpr int T = decltype(Tc)::value, u = lim(T - 1) + decltype(nc)::value;
    if constexpr (u < lim(T)) issue_unit(IC<u>{});
  };
  auto Y = [&](auto Tc, int extra_pos, bool drain_lds) AI {
    constexpr int T = decltype(Tc)::value;
    constexpr int last = T >= 22 ? (T - 22 >= TLAST ? NUNIT - 1 : imin(NUNIT - 1, 50 + (T - 22) + PB)) : cum(T) - 1 == 21 ? 20 : cum(T) - 1;
    int need = pos[last]; if (extra_pos > need) need = extra_pos;
    SB();
    wait_vm(vm - need);
    if (drain_lds) asm volatile("s_waitcnt lgkmcnt(0)" ::: "memory");
    asm volatile("s_barrier" ::: "memory");
    SB();
  };
  sfor<0, lim(-1)>(issue_unit);
  u32 tag;
  cf lam, k_cf, lam8, lam16, lam32, lam8q;
  cf k_a, k_b, k_c, k_l64;
  u32 aS, aT;
  auto consts = [&](auto Sc) AI {
    constexpr int S = decltype(Sc)::value;
    if constexpr (S == 2) {
      const int l = lane_id(), s16 = l & 15;
      aS = nw * 512 + s16 * 8; aT = (w * 64 + l) * 16;
      PIN(aS); PIN(aT);
    } else if constexpr (S == 3) {
      const cf l2 = csqr(lam), l4 = csqr(l2);
      lam8 = csqr(l4); lam16 = csqr(lam8); lam32 = csqr(lam16);
      PINC(lam8); PINC(lam16); PINC(lam32);
    } else if constexpr (S == 4) {
      const int q = lane_id() >> 4;
      const cf one = cf{1.f, 0.f};
      lam8q = cmul(csel(q & 1, lam8, one), csel(q & 2, lam16, one));
      PINC(lam8q);
    } else if constexpr (S == 5) {
      const cf one = cf{1.f, 0.f};
      k_l64 = csqr(lam32);
      k_a = cmul(csel(mw & 1, lam32, one), csel(mw & 2, k_l64, one));
      PINC(k_l64); PINC(k_a);
    } else if constexpr (S == 6) {
      k_a = cmul(lam8q, k_a);
      stsf2(M_CONST + aT, k_a.r, k_a.i);
      k_b = csqr(k_l64);
      k_c = csqr(k_b);
      PINC(k_b); PINC(k_c);
    } else if constexpr (S == 7) {
      const int q = lane_id() >> 4;
      const cf one = cf{1.f, 0.f};
      int ex = g - 1 - (4 * mw + q); ex = ex < 0 ? 0 : ex;
      k_a = cmul(csel(ex & 1, k_b, one), csel(ex & 2, k_c, one));
      k_b = csqr(k_c);
      PINC(k_a); PINC(k_b);
    } else if constexpr (S == 8) {
      const int q = lane_id() >> 4;
      const cf one = cf{1.f, 0.f};
      int ex = g - 1 - (4 * mw + q); ex = ex < 0 ? 0 : ex;
      k_c = csqr(k_b);
      k_a = cmul(k_a, cmul(csel(ex & 4, k_b, one), csel(ex & 8, k_c, one)));
      stsf2(M_CONST + aT + 8, k_a.r, k_a.i);
    }
  };

  u32x2 xr[2], dsk[2];
  u32 aXe;
  {
    const int l = lane_id(), q = l >> 4, s16 = l & 15;
    const int dr = 32 * (mq >> 1) + 8 * (2 * (mq & 1) + (s16 >> 3)) + (s16 & 7);
    aXe = dr * 128 + (((4 * kq + (q >> 1)) ^ ((dr >> 1) & 7)) << 4) + 8 * (q & 1);
    PIN(aXe);
    const u32 c2 = (u32)(64 * nb + 32 * kq + 4 * q) * 2;
    gld8(dsk[0], p.Dsk, c2); gld8(dsk[1], p.Dsk, c2 + 32);
    vm += 2;
  }
  u32x4 xf[28];
  f32x16 accS = {0};
  const char* bPre = p.ws + WS_PRE;
  u32x4 g_st, g_cw, g_lm, g_cf;
  u32x2 g_cb;
  u32 vSt, vCb, vCw, vLm;
  int pos_st = 0, pos_cw = 0;
  {
    const int l = lane_id(), h = l >> 5, r32 = l & 31;
    const int rowx = 32 * (mw & 1) + r32;
    const u32 aX = (mw >> 1) * 8192 + rowx * 128 + ((h ^ ((rowx >> 1) & 7)) << 4);
    const int rowb = 32 * nw + r32;
    const u32 aB = rowb * 128 + ((h ^ ((rowb >> 1) & 7)) << 4);
    u32x4 bf[2][4];
    auto comp = [&](auto Sc, auto hc) AI {
      constexpr int S = decltype(Sc)::value, hh = decltype(hc)::value;
#pragma unroll
      for (int i = 2 * hh; i < 2 * hh + 2; ++i) accS = mfma32(xf[4 * S + i], bf[S & 1][i], accS);
      PIN(accS);
    };
    sfor<0, 7>([&](auto Sc) AI {
      constexpr int S = decltype(Sc)::value;
      Y(Sc, 0, false);
      if constexpr (S == 0) { }
      if constexpr (S == 3) { }
      issue_nth(Sc, IC<0>{});
      SB();
      constexpr int ux = 3 * S + (S & 1), ub = (S & 1) ? 3 * S : 3 * S + 2;
#pragma unroll
      for (int i = 0; i < 4; ++i) {
        xf[4 * S + i] = lds16((aX ^ (32 * i)) + (ux & 15) * 8192);
        bf[S & 1][i] = lds16((aB ^ (32 * i)) + (ub & 15) * 8192);
      }
      SB();
      if constexpr (S > 0) comp(IC<S - 1>{}, IC<0>{});
      SB();
      issue_nth(Sc, IC<1>{});
      SB();
      if constexpr (S == 0) consts(IC<2>{});
      if (S == nb) {
        const int ue = 3 * S + ((S & 1) ? 1 : 0) + mb2;
        const u32 a0 = aXe + (ue & 15) * 8192;
        xr[0] = *(const LAS u32x2*)(uintptr_t)a0;
        xr[1] = *(const LAS u32x2*)(uintptr_t)(a0 ^ 32u);
      }
      if constexpr (S > 0) comp(IC<S - 1>{}, IC<1>{});
      if constexpr (S == 1) {
        asm volatile("global_load_dword %0, %1, %2 sc1" : "=v"(v_ep) : "v"(0u), "s"(p.ws) : "memory");
        vm += 1;
        make_vGU();
      }
      SB();
      issue_nth(Sc, IC<2>{});
      if constexpr (S == PRE_ST) {
        vSt = (u32)(128 * g + 32 * mw + 16 * ((r32 >> 2) & 1) + (r32 & 3) + 4 * (r32 >> 3)) * 16 + PRE_STAT;
        vCb = (u32)((r32 >> 4) * 448 + 32 * j + 16 * nw + (r32 & 15)) * 8 + PRE_CSB;
        gld16_sc1(g_st, bPre, vSt);
        gld8_sc1(g_cb, bPre, vCb);
        vLm = (u32)(32 * j + 16 * nw + (r32 & 15)) * 32 + PRE_LAM;
        gld16_sc1(g_lm, bPre, vLm);
        gld16_sc1(g_cf, bPre, vLm + 16);
        vm += 4;
        pos_st = vm;
      }
      if constexpr (S < 2) { issue_nth(Sc, IC<3>{}); issue_nth(Sc, IC<4>{}); }
    });
    comp(IC<6>{}, IC<0>{});
    comp(IC<6>{}, IC<1>{});
  }

  u32 aW0, aW1, aW0h, aW1h;
  {
    const int l = lane_id(), h = l >> 5, q = l >> 4;
    const int fi = l & 15, fr = fi >> 2, fc4 = fi & 3, fimg = q & 1, fsw = (fr >> 1) | (fimg << 1);
    aW0 = fimg * 8192 + (8 * h + fr) * 128 + (((2 * nw) ^ fsw) << 5) + fc4 * 8;
    aW1 = aW0 ^ 32u;
    aW0h = aW0 + 65536; aW1h = aW1 + 65536;
    PIN(aW0h); PIN(aW1h);
  }
  const char* bH = p.ws + WS_H + (size_t)(4 * g + mw) * NKS * 1024;
  u32x4 wf[2][2];
  auto rdW = [&](int buf, int ug, u32 ko) AI {
    const u32 o = (ug & 7) * 8192 + ko;
    const u32 b0 = (ug & 8) ? aW0h : aW0, b1 = (ug & 8) ? aW1h : aW1;
    wf[buf][0] = ldstr2(b0 + o, b0 + o + 512);
    wf[buf][1] = ldstr2(b1 + o, b1 + o + 512);
  };
  int pos_gran = 0, pos_hs = 0, pos_poll = 0, pos_ha = 0, pos_hb = 0;
  u32x4 gran;
  u32x2 pollv;
  const char* bGran = p.ws + WS_GRAN + (size_t)j * 16 * 512;
  const char* bFlag = p.ws + WS_FLAG + (size_t)g * 14 * 192;
  constexpr int AB = AWIN + 1;
  u32x4 Aw[AB];
  const char* bA = p.ws + WS_H + (size_t)(4 * g + 2 * mb2 + (mq >> 1)) * NKS * 1024;
  u32 vA;
  int posA[42];
  auto issueA = [&](auto tc) AI {
    constexpr int t = decltype(tc)::value;
    if constexpr (t < 42) {
      constexpr int ks = t < 14 ? 8 * t : t < 28 ? 112 + 28 * ((t - 14) & 1) + 4 * ((t - 14) >> 1) : 8 * (t - 28) + 4;
      gld16_sc1(Aw[t % AB], bA + ks * 1024, vA);
      posA[t] = ++vm;
    }
  };
  auto poll_issue = [&](int which) AI {
    pos_poll = vm;
    if (w == 0) {
      const int l = lane_id();
      const u32 fo = which == 3 ? (l < 14 ? l : 0) * 64 : (l < 14 ? l : 0) * 192 + which * 64;
      asm volatile("global_load_dwordx2 %0, %1, %2 sc1" : "=v"(pollv) : "v"(fo), "s"(which == 3 ? bPre + PRE_TF : bFlag) : "memory");
    }
  };
  auto poll_check = [&](int which) AI {
    if (w == 0) {
      wait_vm(vm - pos_poll);
      asm volatile("" : "+v"(pollv));
      const int l = lane_id();
      const u32 fo = which == 3 ? (l < 14 ? l : 0) * 64 : (l < 14 ? l : 0) * 192 + which * 64;
      if (wany(((pollv.x ^ tag) | (pollv.y ^ tag)) != 0)) {
        int spins = 0;
        do { regld8(pollv, which == 3 ? bPre + PRE_TF : bFlag, fo); } while (wany(((pollv.x ^ tag) | (pollv.y ^ tag)) != 0) && ++spins < SPIN_MAX);
      }
    }
  };
  auto flag_store = [&](int which) AI {
    if (threadIdx.x == 0) {
      u32x2 t2 = {tag, tag};
      asm volatile("global_store_dwordx2 %0, %1, %2 sc1" ::"v"((u32)(j * 192 + which * 64)), "v"(t2), "s"(bFlag) : "memory");
    }
  };
  u32 aD[4], aDh[4], aC0, aC0h;
  u32x4 bfr[2][4];
  auto make_p2 = [&]() AI {
    const int l = lane_id(), q = l >> 4, s16 = l & 15;
    const int pr = s16 >> 2, pc4 = l & 3, ph = q & 1, psw = 2 * ph + (pr >> 1);
    const u32 aD0 = (32 * kq + 16 * (q >> 1) + 4 * ph + pr) * 128 + pc4 * 8;
#pragma unroll
    for (int n = 0; n < 4; ++n) aD[n] = aD0 + ((n ^ psw) << 5);
    aC0 = s16 * 128 + (((4 * kq + q) ^ ((s16 >> 1) & 7)) << 4);
#pragma unroll
    for (int n = 0; n < 4; ++n) { aDh[n] = aD[n] + 65536; PIN(aDh[n]); }
    aC0h = aC0 + 65536; PIN(aC0h);
  };
  auto rdB = [&](auto tc) AI {
    constexpr int t = decltype(tc)::value;
    const u32 o = ((50 + t) & 7) * 8192;
    constexpr bool hi = ((50 + t) & 8) != 0;
    if constexpr (t >= 14) {
#pragma unroll
      for (int n = 0; n < 4; ++n) bfr[t & 1][n] = lds16((hi ? aC0h : aC0) + o + n * 2048);
    } else {
#pragma unroll
      for (int n = 0; n < 4; ++n) {
        const u32 a = (hi ? aDh[n] : aD[n]) + o;
        bfr[t & 1][n] = ldstr2(a, a + 1024);
      }
    }
  };

  u32 xm0, xm1;
  cf u[8], e_, A_, T1_, T3_, Pq, Wend, Ploc, H_;
  f32x4 f_a, f_b;
  f32x2 f_cc;
  float f_t0, f_t1;
  f32x2 f_h0, f_h1, f_h2, f_h3, f_h4;
  u32 vGr;
  u32 o_re[4], o_im[4];
  u32x4 fre, fim;
  float o_r0, o_i0;
  auto fill = [&](auto qc) AI {
    constexpr int Q = decltype(qc)::value;
    if constexpr (Q == 0) {
      const int l = lane_id(), r32 = l & 31;
      tag = __builtin_amdgcn_readfirstlane(v_ep) + 1;
      wait_vm(vm - pos_st);
      asm volatile("" : "+v"(g_st), "+v"(g_cb), "+v"(g_lm), "+v"(g_cf));
      if (__builtin_expect(wany((tagbad(g_st, tag) | tagbad(g_lm, tag) | tagbad(g_cf, tag) | (g_cb.y ^ tag)) != 0), 0)) {
        int spins = 0;
        do {
          asm volatile("global_load_dwordx4 %0, %4, %7 sc1\n\tglobal_load_dwordx2 %1, %5, %7 sc1\n\tglobal_load_dwordx4 %2, %6, %7 sc1\n\t"
                       "global_load_dwordx4 %3, %6, %7 offset:16 sc1\n\ts_waitcnt vmcnt(0)"
                       : "+v"(g_st), "+v"(g_cb), "+v"(g_lm), "+v"(g_cf) : "v"(vSt), "v"(vCb), "v"(vLm), "s"(bPre) : "memory");
        } while (wany((tagbad(g_st, tag) | tagbad(g_lm, tag) | tagbad(g_cf, tag) | (g_cb.y ^ tag)) != 0) && ++spins < SPIN_MAX);
      }
      lam = cf{u2f(g_lm.x), u2f(g_lm.z)};
      k_cf = cf{u2f(g_cf.x), u2f(g_cf.z)};
      PINC(lam); PINC(k_cf);
      const float mb = u2f(g_st.x), rs = u2f(g_st.z);
      const int tk4 = 128 * mw + 64 * ((r32 >> 2) & 1) + 4 * (r32 & 3) + 16 * (r32 >> 3);
      stsf(M_STAT + tk4, mb);
      stsf(M_STAT + 512 + tk4, rs);
      const u32 nm = l < 32 ? pack2(-mb, -mb) : 0u;
      xm0 = nm; xm1 = nm & 0xffffu;
      f_t0 = u2f(g_cb.x);
      PIN(xm0); PIN(xm1); PIN(f_t0);
    } else if constexpr (Q == 1) {
      const int q = lane_id() >> 4;
      accS = mfma32(u32x4{xm0, xm1, 0u, 0u}, split3(f_t0), accS);
      PIN(accS);
      consts(IC<3>{});
      f_a = ldsf4(M_STAT + 512 + 128 * mw + 32 * q); f_b = ldsf4(M_STAT + 512 + 128 * mw + 32 * q + 16);
    } else if constexpr (Q == 2) {
      consts(IC<4>{});
    } else if constexpr (Q == 3 || Q == 4) {
#pragma unroll
      for (int i = 4 * (Q - 3); i < 4 * (Q - 3) + 4; ++i) {
        auto sw = __builtin_amdgcn_permlane16_swap(f2u(accS[i]), f2u(accS[i + 8]), false, false);
        const float rr = i < 4 ? f_a[i] : f_b[i - 4];
        u[i] = cf{u2f(sw[0]) * rr, u2f(sw[1]) * rr};
        PINC(u[i]);
      }
    } else if constexpr (Q == 5) {
      e_ = cfma(lam, u[0], u[1]); e_ = cfma(lam, e_, u[2]); e_ = cfma(lam, e_, u[3]); PINC(e_);
    } else if constexpr (Q == 6) {
      e_ = cfma(lam, e_, u[4]); e_ = cfma(lam, e_, u[5]); PINC(e_);
    } else if constexpr (Q == 7) {
      e_ = cfma(lam, e_, u[6]); e_ = cfma(lam, e_, u[7]); PINC(e_);
    } else if constexpr (Q == 8) {
      cf d, T, yo;
      swap16(e_.r, A_.r, d.r); swap16(e_.i, A_.i, d.i);
      T = cfma(lam8, A_, e_);
      swap16(T.r, d.r, yo.r); swap16(T.i, d.i, yo.i);
      swap32(yo.r, T1_.r, T3_.r); swap32(yo.i, T1_.i, T3_.i);
      PINC(A_); PINC(T1_); PINC(T3_);
    } else if constexpr (Q == 9) {
      Wend = cfma(lam16, T1_, T3_);
      stsf2(M_W + mw * 128 + aS, Wend.r, Wend.i);
      PINC(Wend);
    } else if constexpr (Q == 10) {
      const int q = lane_id() >> 4;
      const cf P3 = cfma(lam8, T1_, A_);
      Pq = csel(q & 2, csel(q & 1, P3, T1_), csel(q & 1, A_, cf{0.f, 0.f}));
      PINC(Pq);
    } else if constexpr (Q == 11) {
      const f32x2 w0 = ldsf2(M_W + aS), w1 = ldsf2(M_W + 128 + aS);
      f_cc = ldsf2(M_W + 256 + aS);
      f_a = f32x4{w0.x, w0.y, w1.x, w1.y};
    } else if constexpr (Q == 12) {
      cf Qc = cf{0.f, 0.f};
      if (mw > 0) Qc = cf{f_a.x, f_a.y};
      if (mw > 1) Qc = cfma(lam32, Qc, cf{f_a.z, f_a.w});
      if (mw > 2) Qc = cfma(lam32, Qc, cf{f_cc.x, f_cc.y});
      Ploc = cfma(lam8q, Qc, Pq);
      Wend = cfma(lam32, Qc, Wend);
      PINC(Ploc); PINC(Wend);
    } else if constexpr (Q == 13) {
      if (mw == 3 && lane_id() < 16) {
        u32x4 gv = {f2u(Wend.r), tag, f2u(Wend.i), tag};
        gst16_sc1(bGran + g * 512, (aT & 0xf0u) + nw * 256, gv);
      }
    } else if constexpr (Q >= 14 && Q <= 17) {
      consts(IC<Q - 9>{});
    } else if constexpr (Q == 18) {
      make_vDC();
    } else if constexpr (Q == 19 || Q == 45) {
      const int r32 = lane_id() & 31;
      vCw = (u32)((((j * 2 + (Q == 45)) * 2 + nw) * 32 + r32) * 16) + PRE_CSW;
      gld16_sc1(g_cw, bPre, vCw);
      pos_cw = ++vm;
    } else if constexpr (Q == 20) {
      const int q = lane_id() >> 4;
      vGr = (u32)((4 * mw + q) * 512) + (aS & 0x78u) * 2 + nw * 256;
      PIN(vGr);
      gld16_sc1(gran, bGran, vGr);
      pos_gran = ++vm;
    } else if constexpr (Q == 23) {
      asm volatile("" : "+v"(gran));
      const f32x2 cv = ldsf2(M_CONST + aT + 8);
      const bool need = (vGr >> 9) < (u32)g;
      if (__builtin_expect(wany(need && tagbad(gran, tag) != 0), 0)) {
        int spins = 0;
        do { regld16(gran, bGran, vGr); } while (wany(need && tagbad(gran, tag) != 0) && ++spins < SPIN_MAX);
      }
      H_ = cmul(cf{cv.x, cv.y}, cf{u2f(gran.x), u2f(gran.z)});
      if (!need) H_ = cf{0.f, 0.f};
      PINC(H_);
    } else if constexpr (Q == 24) {
      float a0, a1;
      swap16(H_.r, a0, a1); H_.r = a0 + a1; swap16(H_.i, a0, a1); H_.i = a0 + a1;
      swap32(H_.r, a0, a1); H_.r = a0 + a1; swap32(H_.i, a0, a1); H_.i = a0 + a1;
      stsf2(M_H + mw * 128 + aS, H_.r, H_.i);
    } else if constexpr (Q == 27) {
      f_h0 = ldsf2(M_H + aS); f_h1 = ldsf2(M_H + 128 + aS); f_h2 = ldsf2(M_H + 256 + aS); f_h3 = ldsf2(M_H + 384 + aS);
      f_h4 = ldsf2(M_CONST + aT);
    } else if constexpr (Q == 28) {
      const cf Hin = cf{(f_h0.x + f_h1.x) + (f_h2.x + f_h3.x), (f_h0.y + f_h1.y) + (f_h2.y + f_h3.y)};
      H_ = cfma(cf{f_h4.x, f_h4.y}, Hin, Ploc);
      PINC(H_);
    } else if constexpr (Q >= 29 && Q <= 36) {
      constexpr int i = Q - 29;
      H_ = cfma(lam, H_, u[i]);
      const float orr = __builtin_fmaf(k_cf.r, H_.r, -(k_cf.i * H_.i)), oim = __builtin_fmaf(k_cf.r, H_.i, k_cf.i * H_.r);
      if constexpr ((i & 1) == 0) { o_r0 = orr; o_i0 = oim; PIN(o_r0); PIN(o_i0); }
      else { o_re[i >> 1] = pack2(o_r0, orr); o_im[i >> 1] = pack2(o_i0, oim) ^ 0x80008000u; PIN(o_re[i >> 1]); PIN(o_im[i >> 1]); }
      PINC(H_);
    } else if constexpr (Q == 37) {
      const int l = lane_id(), q = l >> 4, s16 = l & 15;
      const u32 wa = M_TRANS + w * 2048 + s16 * 64 + ((q ^ ((s16 >> 1) & 3)) << 4);
      *(LAS u32x4*)(uintptr_t)(wa) = u32x4{o_re[0], o_re[1], o_re[2], o_re[3]};
      *(LAS u32x4*)(uintptr_t)(wa + 1024) = u32x4{o_im[0], o_im[1], o_im[2], o_im[3]};
    } else if constexpr (Q == 39) {
      const int l = lane_id(), h = l >> 5, q = l >> 4, s16 = l & 15, fr = s16 >> 2, fc4 = l & 3;
      const u32 tb = M_TRANS + w * 2048;
      const int gg = q & 1, tc = 2 * (fc4 & 1) + gg;
      const int st0 = 8 * h + fr;
      const u32 ra0 = tb + st0 * 64 + ((tc ^ ((st0 >> 1) & 3)) << 4) + (fc4 >> 1) * 8;
      const u32 ra1 = (ra0 + 256) ^ 32u;
      fre = ldstr2(ra0, ra1); fim = ldstr2(ra0 + 1024, ra1 + 1024);
    } else if constexpr (Q == 40) {
      const u32 vL16 = aT & 1023u;
      gst16_sc1(bH + (112 + 2 * j + nw) * 1024, vL16, fre);
      gst16_sc1(bH + (140 + 2 * j + nw) * 1024, vL16, fim);
      vm += 2;
      pos_hs = vm;
    } else if constexpr (Q == 44) {
      poll_issue(0);
    } else if constexpr (Q == 48) {
      const int l = lane_id(), q = l >> 4, s16 = l & 15;
      vA = (q >> 1) * 1024 + (q & 1) * 512 + (16 * (mq & 1) + s16) * 16 + kq * 2048;
      PIN(vA);
    } else if constexpr (Q == 52) {
      make_p2();
    }
  };

  Y(IC<7>{}, 0, false);

  rdW(0, 22, 0);
  issue_nth(IC<7>{}, IC<0>{});
  sfor<0, 2>([&](auto Pc) AI {
    constexpr int P = decltype(Pc)::value;
    constexpr int S0 = 7 + 7 * P, U0 = 22 + 14 * P;
    f32x16 acc0 = {0}, acc1 = {0};
    sfor<0, 7>([&](auto kbc) AI {
      constexpr int kb = decltype(kbc)::value;
      constexpr int S = S0 + kb, ug = U0 + 2 * kb;
      sfor<0, 4>([&](auto kslc) AI {
        constexpr int ksl = decltype(kslc)::value, Q = 4 * (S - 7) + ksl;
        const u32x4 xv = xf[4 * kb + ksl];
        SB();

        acc0 = mfma32(wf[ksl & 1][0], xv, acc0);
        PIN(acc0);
        SB();
        if constexpr (ksl < 3) rdW((ksl + 1) & 1, ug, (ksl + 1) * 2048);
        else if constexpr (S < 20) { const u32x4 keep = wf[1][1]; rdW(0, ug + 2, 0); wf[1][1] = keep; }
        else rdB(IC<0>{});
        SB();
        acc1 = mfma32(wf[ksl & 1][1], xv, acc1);
        PIN(acc1);
        SB();
        fill(IC<Q>{});
        if constexpr (ksl == 3 && kb == 6) {
          wait_vm(vm - pos_cw);
          asm volatile("" : "+v"(g_cw));
          if (__builtin_expect(wany(tagbad(g_cw, tag) != 0), 0)) {
            int spins = 0;
            do { regld16(g_cw, bPre, vCw); } while (wany(tagbad(g_cw, tag) != 0) && ++spins < SPIN_MAX);
          }
          f_t0 = u2f(g_cw.x); f_t1 = u2f(g_cw.z);
        }
        SB();
        if constexpr (ksl == 3) issue_nth(IC<S + 1>{}, IC<0>{});
        if constexpr (ksl == 0) issue_nth(IC<S>{}, IC<2>{});
        if constexpr (ksl == 1) issue_nth(IC<S>{}, IC<1>{});
        if constexpr (Q >= 51) issueA(IC<Q - 51>{});
        if constexpr (ksl == 2) {
          int xp = 0;
          if (Q == 22) xp = pos_gran;
          if (Q == 34) xp = pos_ha;
          if (Q == 46) xp = pos_hs;
          if constexpr (Q == 50) poll_check(0);
          Y(IC<S + 1>{}, xp, Q == 10 || Q == 26);
          if constexpr (Q == 34) flag_store(0);
          if constexpr (Q == 46) flag_store(1);
        }
      });
    });

    {
      const u32x4 xm = u32x4{xm0, xm1, 0u, 0u};
      acc0 = mfma32(split3(f_t0), xm, acc0);
      acc1 = mfma32(split3(f_t1), xm, acc1);
    }
    {
      u32x4 o0, o1;
      const int ks = 8 * j + 4 * P + 2 * nw;
      const u32 vL16 = aT & 1023u;
#pragma unroll
      for (int i = 0; i < 4; ++i) o0[i] = pack2(fmaxf(acc0[2 * i], 0.f) * acc0[2 * i + 8], fmaxf(acc0[2 * i + 1], 0.f) * acc0[2 * i + 9]);
      gst16_sc1(bH + ks * 1024, vL16, o0);
#pragma unroll
      for (int i = 0; i < 4; ++i) o1[i] = pack2(fmaxf(acc1[2 * i], 0.f) * acc1[2 * i + 8], fmaxf(acc1[2 * i + 1], 0.f) * acc1[2 * i + 9]);
      gst16_sc1(bH + ks * 1024 + 1024, vL16, o1);
      vm += 2;
      if constexpr (P == 0) pos_ha = vm; else pos_hb = vm;
    }

    if constexpr (P == 1) { issue_nth(IC<21>{}, IC<1>{}); issue_nth(IC<21>{}, IC<2>{}); sfor<5, AWIN>(issueA); }
  });

  f32x4 af[4], as[4];
#pragma unroll
  for (int i = 0; i < 4; ++i) { af[i] = f32x4{0, 0, 0, 0}; as[i] = f32x4{0, 0, 0, 0}; }
  float e_x[8], e_sk[8], e_rs2, e_mb, e_rs;
  int tok, col0;
  sfor<0, 42>([&](auto tc) AI {
    constexpr int t = decltype(tc)::value, T = 22 + t / PB * PB;
    constexpr int PC1 = (14 - AWIN) / PB * PB, PC2 = (28 - AWIN) / PB * PB, PC3 = 14 / PB * PB;
    if constexpr (t % PB == 0 && t <= TLAST) {
      int xp = posA[t];
      if (t == 4 && pos_hb > xp) xp = pos_hb;
      if constexpr (t == PC1) poll_check(1);
      if constexpr (t == PC2) poll_check(2);
      if constexpr (t == PC3) poll_check(3);
      Y(IC<T>{}, xp, false);
      if constexpr (t == 4) flag_store(2);
    } else {
      SB(); wait_vm(vm - posA[t]); SB();
    }
    issueA(IC<t + AWIN>{});
    if constexpr (t == 0) poll_issue(1);
    if constexpr (t == PC3 - 4) poll_issue(3);
    if constexpr (t == PC3) poll_issue(2);
    SB();
    if constexpr (t + 1 < 42) rdB(IC<t + 1>{});
    SB();
    asm volatile("" : "+v"(Aw[t % AB]));
    const u32x4 av = Aw[t % AB];
    if constexpr (t >= 14 && t < 28) {
#pragma unroll
      for (int n = 0; n < 4; ++n) { as[n] = mfma16(bfr[t & 1][n], av, as[n]); PIN(as[n]); }
    } else {
#pragma unroll
      for (int n = 0; n < 4; ++n) { af[n] = mfma16(bfr[t & 1][n], av, af[n]); PIN(af[n]); }
    }
    if constexpr (t == 32) {
      const int l = lane_id(), q = l >> 4, s16 = l & 15;
      tok = 64 * mb2 + 32 * (mq >> 1) + 16 * ((s16 >> 2) & 1) + (s16 & 3) + 8 * (mq & 1) + 4 * (s16 >> 3);
      col0 = 64 * nb + 32 * kq + 4 * q;
    }
    if constexpr (t == 33) {
      asm volatile("" : "+v"(xr[0]), "+v"(xr[1]), "+v"(dsk[0]), "+v"(dsk[1]));
      e_mb = *(const LAS float*)(uintptr_t)(M_STAT + tok * 4); e_rs = *(const LAS float*)(uintptr_t)(M_STAT + 512 + tok * 4);
      e_rs2 = e_rs * e_rs;
      PIN(e_mb); PIN(e_rs); PIN(e_rs2);
    }
    if constexpr (t >= 34) {
      constexpr int i = t - 34;
      const u32 xw = xr[i >> 2][(i >> 1) & 1], dw = dsk[i >> 2][(i >> 1) & 1];
      const float xv = (i & 1) ? bfhi(xw) : bf2f(xw), dv = (i & 1) ? bfhi(dw) : bf2f(dw);
      const float xn = rbf(rbf(xv - e_mb) * e_rs);
      e_x[i] = xv; e_sk[i] = rbf(xn * dv);
      PIN(e_x[i]); PIN(e_sk[i]);
    }
    SB();
    issue_nth(IC<T>{}, IC<t % PB>{});
  });
  {
    const int l = lane_id();
    const u32 xb = 12 * 8192 + (mq * 2 + (kq ^ 1)) * 4096 + l * 16, rb = 12 * 8192 + (mq * 2 + kq) * 4096 + l * 16;
    const f32x4 sf0 = kq ? af[0] : af[2], sf1 = kq ? af[1] : af[3], ss0 = kq ? as[0] : as[2], ss1 = kq ? as[1] : as[3];
    *(LAS f32x4*)(uintptr_t)(xb) = sf0; *(LAS f32x4*)(uintptr_t)(xb + 1024) = sf1;
    *(LAS f32x4*)(uintptr_t)(xb + 2048) = ss0; *(LAS f32x4*)(uintptr_t)(xb + 3072) = ss1;
    asm volatile("s_waitcnt lgkmcnt(0)\n\ts_barrier" ::: "memory");
    f32x4 kf[2] = {kq ? af[2] : af[0], kq ? af[3] : af[1]}, ks[2] = {kq ? as[2] : as[0], kq ? as[3] : as[1]};
    const f32x4 rf[2] = {ldsf4(rb), ldsf4(rb + 1024)}, rs[2] = {ldsf4(rb + 2048), ldsf4(rb + 3072)};
#pragma unroll
    for (int n = 0; n < 2; ++n) {
      f32x4 o;
#pragma unroll
      for (int h = 0; h < 4; h += 2) {
        const int i = 4 * n + h;
        const float y0 = ks[n][h] + rs[n][h] + e_sk[i], y1 = ks[n][h + 1] + rs[n][h + 1] + e_sk[i + 1];
        u32 fp = pack2((kf[n][h] + rf[n][h]) * e_rs2, (kf[n][h + 1] + rf[n][h + 1]) * e_rs2);
        asm("" : "+v"(fp));
        const float f0 = bf2f(fp), f1 = bfhi(fp);
        u32 gp = pack2(f0 * __builtin_amdgcn_rcpf(1.f + __builtin_amdgcn_exp2f(-1.44269504f * f0)),
                       f1 * __builtin_amdgcn_rcpf(1.f + __builtin_amdgcn_exp2f(-1.44269504f * f1)));
        asm("" : "+v"(gp));
        o[h] = __builtin_fmaf(y0, bf2f(gp), e_x[i]); o[h + 1] = __builtin_fmaf(y1, bfhi(gp), e_x[i + 1]);
      }
      asm volatile("global_store_dwordx4 %0, %1, %2 " OUTS "\n\ts_nop 1" ::"v"((u32)(((128 * g + tok) * 448 + col0 + 16 * n) * 4)), "v"(o), "s"(p.out) : "memory");
    }
  }
  if (g == 15 && j == 0 && threadIdx.x == 0) *(volatile u32*)p.ws = tag;
  asm volatile(".Lpf_end:" ::: "memory");
}

extern "C" void kernel_launch(void* const* d_in, const int* in_sizes, int n_in, void* d_out, int out_size, void* d_ws,
                              size_t ws_size, hipStream_t stream) {
  Params p;
  p.x = (const u16*)d_in[0]; p.lre = (const u16*)d_in[1]; p.lim = (const u16*)d_in[2];
  p.Bre = (const u16*)d_in[3]; p.Bim = (const u16*)d_in[4]; p.Cre = (const u16*)d_in[5]; p.Cim = (const u16*)d_in[6];
  p.ldt = (const u16*)d_in[7]; p.Dsk = (const u16*)d_in[8];
  p.Wg = (const u16*)d_in[10]; p.Wu = (const u16*)d_in[11]; p.Wd = (const u16*)d_in[12];
  p.out = (float*)d_out; p.ws = (char*)d_ws;
  (void)hipFuncSetAttribute((const void*)fused_ssm_ffn, hipFuncAttributeMaxDynamicSharedMemorySize, LDS_BYTES);
  fused_ssm_ffn<<<224 + NHELP, 512, LDS_BYTES, stream>>>(p);
}
```
